```python
import torch

def _softmax(x, axis=-1):
    return torch.softmax(x, dim=axis)
def _gelu(x):
    return torch.nn.functional.gelu(x, approximate="tanh")
def _silu(x):
    return torch.nn.functional.silu(x)
def _sigmoid(x):
    return torch.sigmoid(x)
def _layernorm(x, axis=-1, eps=1e-5):
    m = x.mean(dim=axis, keepdim=True)
    v = x.var(dim=axis, keepdim=True, unbiased=False)
    return (x - m) / torch.sqrt(v + eps)
def _scatter_add(tgt, idx, src, axis=0):
    return tgt.clone().index_add_(axis, idx, src)
def _segment_sum(src, seg, n):
    out = torch.zeros((n,) + src.shape[1:], dtype=src.dtype)
    return out.index_add_(0, seg, src)
def _pad(x, pw):
    flat = tuple(v for pair in reversed(pw) for v in pair)
    return torch.nn.functional.pad(x, flat)

kernel_name = 'standalone__qk_norm__prompt'

def setup_inputs():

    _w = (lambda a: (lambda t: t.to(torch.bfloat16) if t.is_floating_point() else t)(torch.as_tensor(a)))
    B, H, S, D = 1, 16, 2048, 448
    Dh = D // H
    return {
        "q": _w((lambda *s: torch.randn(*s, dtype=torch.bfloat16) * 0.05)(B, H, S, Dh)),
        "k": _w((lambda *s: torch.randn(*s, dtype=torch.bfloat16) * 0.05)(B, H, S, Dh)),
        "v": _w((lambda *s: torch.randn(*s, dtype=torch.bfloat16) * 0.8)(B, H, S, Dh)),
        "t": _w(torch.abs((lambda *s: torch.randn(*s, dtype=torch.bfloat16) * 0.8)(H)) + 1.0),
    }

def reference(q, k, v, t):
    eps = 1e-6
    qn = q / (torch.sqrt((q * q).sum(axis=-1, keepdims=True)) + eps)
    kn = k / (torch.sqrt((k * k).sum(axis=-1, keepdims=True)) + eps)
    logits = torch.einsum('bhqd,bhkd->bhqk', qn, kn) * t[None, :, None, None]
    attn = _softmax(logits, axis=-1)
    return torch.einsum('bhqk,bhkd->bhqd', attn, v)
```

```cpp
#include <hip/hip_runtime.h>
#include <hip/hip_bf16.h>
#include <stdint.h>

constexpr int S_ = 2048, DH = 28, NH = 16;

__device__ __forceinline__ float bflo(unsigned x) { return __uint_as_float(x << 16); }
__device__ __forceinline__ float bfhi(unsigned x) { return __uint_as_float(x & 0xffff0000u); }

__device__ __forceinline__ void slow_path(const unsigned* __restrict__ Q, const unsigned* __restrict__ K,
                                          const unsigned* __restrict__ V, float t, unsigned* __restrict__ O,
                                          int head, int qblk) {
    const int tid = threadIdx.x;
    const int qrow = qblk * 128 + (tid >> 2), part = tid & 3;
    const unsigned* qp = Q + (size_t)(head * S_ + qrow) * 14;
    float q[DH], o[DH];
    float n = 0.f;
#pragma unroll
    for (int i = 0; i < 14; i++) { unsigned x = qp[i]; q[2 * i] = bflo(x); q[2 * i + 1] = bfhi(x); }
#pragma unroll
    for (int i = 0; i < DH; i++) { n += q[i] * q[i]; o[i] = 0.f; }
    const float rq = 1.0f / (sqrtf(n) + 1e-6f);
#pragma unroll
    for (int i = 0; i < DH; i++) q[i] *= rq;
    float m = -INFINITY, l = 0.f;
#pragma unroll 1
    for (int kk = part; kk < S_; kk += 4) {
        const unsigned* kp = K + (size_t)(head * S_ + kk) * 14;
        const unsigned* vp = V + (size_t)(head * S_ + kk) * 14;
        float dot = 0.f, kn = 0.f;
#pragma unroll
        for (int i = 0; i < 14; i++) {
            unsigned x = kp[i]; float a = bflo(x), b = bfhi(x);
            dot += q[2 * i] * a + q[2 * i + 1] * b; kn += a * a + b * b;
        }
        const float s = dot / (sqrtf(kn) + 1e-6f) * t;
        const float mn = fmaxf(m, s);
        const float alpha = __expf(m - mn), p = __expf(s - mn);
        m = mn; l = l * alpha + p;
#pragma unroll
        for (int i = 0; i < 14; i++) {
            unsigned x = vp[i];
            o[2 * i] = o[2 * i] * alpha + p * bflo(x); o[2 * i + 1] = o[2 * i + 1] * alpha + p * bfhi(x);
        }
    }
    float mg = fmaxf(m, __shfl_xor(m, 1)); mg = fmaxf(mg, __shfl_xor(mg, 2));
    const float f = __expf(m - mg);
    l *= f; l += __shfl_xor(l, 1); l += __shfl_xor(l, 2);
    const float inv = 1.0f / l;
    unsigned* op = O + (size_t)(head * S_ + qrow) * 14;
#pragma unroll
    for (int i = 0; i < 14; i++) {
        float a = o[2 * i] * f, b = o[2 * i + 1] * f;
        a += __shfl_xor(a, 1); a += __shfl_xor(a, 2);
        b += __shfl_xor(b, 1); b += __shfl_xor(b, 2);
        __hip_bfloat16 x = __float2bfloat16(a * inv), y = __float2bfloat16(b * inv);
        unsigned r = (unsigned)__builtin_bit_cast(unsigned short, x) | ((unsigned)__builtin_bit_cast(unsigned short, y) << 16);
        if ((i & 3) == part) op[i] = r;
    }
}

__global__ void __launch_bounds__(512) qkn_kernel(const unsigned* __restrict__ Q, const unsigned* __restrict__ K,
                                                  const unsigned* __restrict__ V, const unsigned short* __restrict__ T,
                                                  unsigned* __restrict__ O) {
    const int bid = blockIdx.x;
    const int head = (bid & 7) * 2 + ((bid >> 3) & 1), qblk = bid >> 4;
    const float t = __uint_as_float((unsigned)T[head] << 16);
    slow_path(Q, K, V, t, O, head, qblk);
}

extern "C" void kernel_launch(void* const* d_in, const int* in_sizes, int n_in, void* d_out, int out_size,
                              void* d_ws, size_t ws_size, hipStream_t stream) {
    qkn_kernel<<<256, 512, 0, stream>>>((const unsigned*)d_in[0], (const unsigned*)d_in[1], (const unsigned*)d_in[2],
                                        (const unsigned short*)d_in[3], (unsigned*)d_out);
}
```

```cpp
#include <hip/hip_runtime.h>
#include <hip/hip_bf16.h>
#include <stdint.h>

constexpr int S_ = 2048, DH = 28;

__device__ __forceinline__ float bflo(unsigned x) { return __uint_as_float(x << 16); }
__device__ __forceinline__ float bfhi(unsigned x) { return __uint_as_float(x & 0xffff0000u); }

__device__ __forceinline__ void generic_path(const unsigned* __restrict__ Q, const unsigned* __restrict__ K,
                                             const unsigned* __restrict__ V, float t, unsigned* __restrict__ O,
                                             int head, int qblk) {
    const int tid = threadIdx.x;
    const int qrow = qblk * 128 + (tid >> 2), part = tid & 3;
    const unsigned* qp = Q + (size_t)(head * S_ + qrow) * 14;
    float q[DH], o[DH];
    float n = 0.f;
#pragma unroll
    for (int i = 0; i < 14; i++) { unsigned x = qp[i]; q[2 * i] = bflo(x); q[2 * i + 1] = bfhi(x); }
#pragma unroll
    for (int i = 0; i < DH; i++) { n += q[i] * q[i]; o[i] = 0.f; }
    const float rq = 1.0f / (sqrtf(n) + 1e-6f);
#pragma unroll
    for (int i = 0; i < DH; i++) q[i] *= rq;
    float m = -INFINITY, l = 0.f;
#pragma unroll 1
    for (int kk = part; kk < S_; kk += 4) {
        const unsigned* kp = K + (size_t)(head * S_ + kk) * 14;
        const unsigned* vp = V + (size_t)(head * S_ + kk) * 14;
        float dot = 0.f, kn = 0.f;
#pragma unroll
        for (int i = 0; i < 14; i++) {
            unsigned x = kp[i]; float a = bflo(x), b = bfhi(x);
            dot += q[2 * i] * a + q[2 * i + 1] * b; kn += a * a + b * b;
        }
        const float s = dot / (sqrtf(kn) + 1e-6f) * t;
        const float mn = fmaxf(m, s);
        const float alpha = __expf(m - mn), p = __expf(s - mn);
        m = mn; l = l * alpha + p;
#pragma unroll
        for (int i = 0; i < 14; i++) {
            unsigned x = vp[i];
            o[2 * i] = o[2 * i] * alpha + p * bflo(x); o[2 * i + 1] = o[2 * i + 1] * alpha + p * bfhi(x);
        }
    }
    float mg = fmaxf(m, __shfl_xor(m, 1)); mg = fmaxf(mg, __shfl_xor(mg, 2));
    const float f = __expf(m - mg);
    l *= f; l += __shfl_xor(l, 1); l += __shfl_xor(l, 2);
    const float inv = 1.0f / l;
    unsigned* op = O + (size_t)(head * S_ + qrow) * 14;
#pragma unroll
    for (int i = 0; i < 14; i++) {
        float a = o[2 * i] * f, b = o[2 * i + 1] * f;
        a += __shfl_xor(a, 1); a += __shfl_xor(a, 2);
        b += __shfl_xor(b, 1); b += __shfl_xor(b, 2);
        __hip_bfloat16 x = __float2bfloat16(a * inv), y = __float2bfloat16(b * inv);
        unsigned r = (unsigned)__builtin_bit_cast(unsigned short, x) | ((unsigned)__builtin_bit_cast(unsigned short, y) << 16);
        if ((i & 3) == part) op[i] = r;
    }
}


__global__ void __launch_bounds__(512) qkn_kernel(const unsigned* __restrict__ Q, unsigned qn, unsigned qf,
                                                  const unsigned* __restrict__ K, unsigned kn, unsigned kf,
                                                  const unsigned* __restrict__ V, const unsigned* __restrict__ T,
                                                  unsigned* __restrict__ O, unsigned* __restrict__ WS) {
    __shared__ __attribute__((aligned(256))) char lds[147456];
    const int bid = blockIdx.x;
    typedef const __attribute__((address_space(4))) unsigned long long* karg_t;
    karg_t ka = (karg_t)__builtin_amdgcn_kernarg_segment_ptr();
    asm volatile(
        "s_load_dwordx16 s[36:51], %[ka], 0\n\t"
        "v_lshrrev_b32 v96, 6, %[tid]\n\t"
        "v_and_b32 v97, 31, %[tid]\n\t"
        "s_and_b32 s31, %[bid], 15\n\t"
        "v_readfirstlane_b32 s20, v96\n\t"
        "s_getpc_b64 s[16:17]\n\t"
        "v_lshlrev_b32 v1, 4, %[tid]\n\t"
        "v_min_u32 v1, 6784, v1\n\t"
        "global_load_dword v1, v1, s[16:17]\n\t"
        "v_bfe_u32 v98, %[tid], 5, 1\n\t"
        "v_mul_u32_u24 v14, 56, v97\n\t"
        "s_lshr_b32 s21, %[bid], 4\n\t"
        "s_and_b32 s25, s20, 3\n\t"
        "s_lshr_b32 s29, s20, 2\n\t"
        "v_mad_u32_u24 v2, v98, 28, v14\n\t"
        "s_xor_b32 s29, s29, 1\n\t"
        "s_lshl1_add_u32 s27, s25, s29\n\t"
        "s_lshl4_add_u32 s26, s31, s21\n\t"
        "s_lshl3_add_u32 s30, s31, s27\n\t"
        "s_lshl2_add_u32 s26, s26, s25\n\t"
        "s_and_b32 s18, s21, 3\n\t"
        "s_mul_i32 s26, s26, 1792\n\t"
        "s_lshl2_add_u32 s24, s30, s18\n\t"
        "s_mul_i32 s58, s24, 3584\n\t"
        "s_setprio 3\n\t"
        "s_cmp_ge_u32 s20, 4\n\t"
        "s_cbranch_scc1 L_lda%=\n\t"
        "s_waitcnt lgkmcnt(0)\n\t"
        "buffer_load_dwordx4 v[224:227], v2, s[40:43], s58 offen offset:0\n\t"
        "buffer_load_dwordx3 v[228:230], v2, s[40:43], s58 offen offset:16\n\t"
        "buffer_load_dwordx4 v[232:235], v2, s[40:43], s58 offen offset:1792\n\t"
        "buffer_load_dwordx3 v[236:238], v2, s[40:43], s58 offen offset:1808\n\t"
        "s_setprio 2\n\t"
        "v_mov_b64 v[80:81], 0\n\t"
        "v_and_b32 v99, 63, %[tid]\n\t"
        "v_and_b32 v101, 3, %[tid]\n\t"
        "v_mfma_f32_32x32x8_bf16 v[64:79], v[80:81], v[80:81], 0\n\t"
        "s_lshl_b32 s27, s27, 14\n\t"
        "v_lshlrev_b32 v3, 4, v99\n\t"
        "v_lshlrev_b32 v96, 4, %[tid]\n\t"
        "s_mov_b32 m0, s27\n\t"
        "v_add_u32 v96, 139264, v96\n\t"
        "v_mov_b32 v108, 0x3f80\n\t"
        "v_mov_b32 v109, 0\n\t"
        "s_mov_b64 s[56:57], s[46:47]\n\t"
        "s_mov_b64 s[46:47], s[42:43]\n\t"
        "s_and_b32 s22, %[bid], 14\n\t"
        "v_mfma_f32_32x32x8_bf16 v[16:31], v[80:81], v[80:81], 0\n\t"
        "s_lshl_b32 s22, s22, 1\n\t"
        "ds_write2_b64 v96, v[108:109], v[108:109] offset1:1\n\t"
        "v_lshl_add_u32 v96, v101, 12, s27\n\t"
        "s_branch L_ld%=\n\t"
        "L_rq0%=:\n\t"
        "s_setprio 1\n\t"
        "L_rq%=:\n\t"
        "ds_read_b32 v96, v110 offset:8164\n\t"
        "ds_read_b128 v[192:195], v104 offset:0\n\t"
        "ds_read_b128 v[196:199], v104 offset:1024\n\t"
        "s_waitcnt lgkmcnt(0)\n\t"
        "v_cmp_eq_u32 vcc, 0, v96\n\t"
        "s_nop 0\n\t"
        "s_cmp_eq_u64 vcc, 0\n\t"
        "s_cbranch_scc1 L_gotq%=\n\t"
        "s_sleep 1\n\t"
        "s_branch L_rq%=\n\t"
        "L_pub%=:\n\t"
        "buffer_store_dwordx4 v[224:227], v4, s[52:55], s62 offen offset:0 sc1\n\t"
        "buffer_store_dwordx4 v[228:231], v4, s[52:55], s62 offen offset:1024 sc1\n\t"
        "buffer_store_dwordx4 v[232:235], v4, s[52:55], s62 offen offset:2048 sc1\n\t"
        "buffer_store_dwordx4 v[236:239], v4, s[52:55], s62 offen offset:3072 sc1\n\t"
        "s_branch L_pubd%=\n\t"
        "L_flag%=:\n\t"
        "s_waitcnt vmcnt(0)\n\t"
        "v_add_u32 v1, 1, v13\n\t"
        "s_lshl_b64 exec, 1, s18\n\t"
        "buffer_store_dword v1, v15, s[52:55], s30 offen sc1\n\t"
        "s_mov_b64 exec, -1\n\t"
        "s_branch L_flagd%=\n\t"
        "s_nop 0\n\t"
        "s_nop 0\n\t"
        "s_nop 0\n\t"
        "s_nop 0\n\t"
        "s_nop 0\n\t"
        "s_nop 0\n\t"
        "s_nop 0\n\t"
        "L_lda%=:\n\t"
        "s_waitcnt lgkmcnt(0)\n\t"
        "buffer_load_dwordx4 v[112:115], v2, s[36:39], s26 offen offset:0\n\t"
        "buffer_load_dwordx3 v[116:118], v2, s[36:39], s26 offen offset:16\n\t"
        "v_mov_b64 v[80:81], 0\n\t"
        "v_and_b32 v99, 63, %[tid]\n\t"
        "v_and_b32 v101, 3, %[tid]\n\t"
        "v_mfma_f32_32x32x8_bf16 v[64:79], v[80:81], v[80:81], 0\n\t"
        "s_lshl_b32 s27, s27, 14\n\t"
        "v_lshlrev_b32 v3, 4, v99\n\t"
        "v_lshlrev_b32 v96, 4, %[tid]\n\t"
        "s_mov_b32 m0, s27\n\t"
        "v_add_u32 v96, 139264, v96\n\t"
        "v_mov_b32 v108, 0x3f80\n\t"
        "v_mov_b32 v109, 0\n\t"
        "s_mov_b64 s[56:57], s[46:47]\n\t"
        "s_mov_b64 s[46:47], s[42:43]\n\t"
        "s_and_b32 s22, %[bid], 14\n\t"
        "v_mfma_f32_32x32x8_bf16 v[16:31], v[80:81], v[80:81], 0\n\t"
        "s_lshl_b32 s22, s22, 1\n\t"
        "ds_write2_b64 v96, v[108:109], v[108:109] offset1:1\n\t"
        "v_lshl_add_u32 v96, v101, 12, s27\n\t"
        "buffer_load_dwordx4 v[224:227], v2, s[40:43], s58 offen offset:0\n\t"
        "buffer_load_dwordx3 v[228:230], v2, s[40:43], s58 offen offset:16\n\t"
        "buffer_load_dwordx4 v[232:235], v2, s[40:43], s58 offen offset:1792\n\t"
        "buffer_load_dwordx3 v[236:238], v2, s[40:43], s58 offen offset:1808\n\t"
        "L_ld%=:\n\t"
        "s_lshl3_add_u32 s24, s25, 139264\n\t"
        "ds_write_b32 v96, v80 offset:4080\n\t"
        "v_add_u32 v100, 131072, v3\n\t"
        "v_lshlrev_b32 v110, 3, v101\n\t"
        "v_mov_b32 v111, s24\n\t"
        "v_add_u32 v110, 139264, v110\n\t"
        "s_lshl_b32 s29, s29, 14\n\t"
        "v_mfma_f32_32x32x8_bf16 v[32:47], v[80:81], v[80:81], 0\n\t"
        "v_add_u32 v4, s27, v3\n\t"
        "v_add_u32 v97, s29, v3\n\t"
        "s_waitcnt lgkmcnt(0)\n\t"
        "s_load_dword s22, s[56:57], s22\n\t"
        "v_lshl_add_u32 v107, s25, 11, v100\n\t"
        "s_add_u32 s25, s25, 1\n\t"
        "s_and_b32 s25, s25, 3\n\t"
        "v_lshl_add_u32 v104, s25, 11, v100\n\t"
        "v_lshl_add_u32 v12, s25, 15, v97\n\t"
        "s_add_u32 s25, s25, 1\n\t"
        "s_and_b32 s25, s25, 3\n\t"
        "v_lshl_add_u32 v105, s25, 11, v100\n\t"
        "v_mfma_f32_32x32x8_bf16 v[128:143], v[80:81], v[80:81], 0\n\t"
        "v_lshl_add_u32 v11, s25, 15, v97\n\t"
        "s_add_u32 s25, s25, 1\n\t"
        "s_and_b32 s25, s25, 3\n\t"
        "v_lshl_add_u32 v106, s25, 11, v100\n\t"
        "v_lshl_add_u32 v10, s25, 15, v97\n\t"
        "v_bfe_u32 v96, %[tid], 2, 2\n\t"
        "s_mul_i32 s23, s30, 14336\n\t"
        "v_lshl_add_u32 v96, v98, 3, v96\n\t"
        "s_lshl_b32 s31, s31, 17\n\t"
        "v_mad_u32_u24 v96, v96, 56, s27\n\t"
        "v_mfma_f32_32x32x8_bf16 v[144:159], v[80:81], v[80:81], 0\n\t"
        "s_lshl_b32 s30, s30, 9\n\t"
        "v_lshl_add_u32 v96, v101, 3, v96\n\t"
        "s_add_u32 s30, s30, 2097152\n\t"
        "v_and_b32 v97, 16, %[tid]\n\t"
        "s_mov_b64 s[52:53], s[50:51]\n\t"
        "v_lshl_add_u32 v96, v97, 1, v96\n\t"
        "s_mov_b32 s54, 2162688\n\t"
        "v_lshlrev_b32 v97, 6, v98\n\t"
        "s_mov_b32 s55, 0x20000\n\t"
        "v_sub_u32 v97, 139488, v97\n\t"
        "v_mfma_f32_32x32x8_bf16 v[160:175], v[80:81], v[80:81], 0\n\t"
        "s_lshl_b32 s24, s18, 12\n\t"
        "v_and_b32 v100, 19, %[tid]\n\t"
        "s_add_u32 s62, s24, s31\n\t"
        "v_cmp_eq_u32 vcc, 19, v100\n\t"
        "s_xor_b32 s24, s20, s21\n\t"
        "v_add_u32 v100, 8192, v96\n\t"
        "s_lshr_b32 s24, s24, 2\n\t"
        "v_mov_b32 v7, 0xdddddddd\n\t"
        "s_and_b32 s28, s20, 1\n\t"
        "v_cndmask_b32 v5, v96, v97, vcc\n\t"
        "v_mfma_f32_32x32x8_bf16 v[176:191], v[80:81], v[80:81], 0\n\t"
        "s_xor_b32 s28, s28, s24\n\t"
        "v_lshl_add_u32 v14, v98, 3, v14\n\t"
        "v_cndmask_b32 v6, v100, v97, vcc\n\t"
        "v_add_u32 v14, s27, v14\n\t"
        "v_add_u32 v14, 12288, v14\n\t"
        "v_lshlrev_b32 v15, 7, v101\n\t"
        "v_mov_b32 v231, v108\n\t"
        "v_mov_b32 v239, v108\n\t"
        "v_mov_b32 v55, v108\n\t"
        "v_mov_b32 v63, v108\n\t"
        "s_barrier\n\t"
        "s_waitcnt lgkmcnt(0)\n\t"
        "s_bitcmp1_b32 %[bid], 0\n\t"
        "s_cselect_b32 s24, 0, 16\n\t"
        "s_lshl_b32 s22, s22, s24\n\t"
        "s_and_b32 s22, s22, 0xffff0000\n\t"
        "s_and_b32 s24, s22, 0x7fffffff\n\t"
        "s_cmp_gt_u32 s24, 0x41800000\n\t"
        "s_cbranch_scc1 L_generic%=\n\t"
        "s_cmp_lg_u32 %[lds], 0\n\t"
        "s_cbranch_scc1 L_generic%=\n\t"
        "s_mov_b64 exec, 15\n\t"
        "buffer_load_dword v13, v15, s[52:55], s30 offen sc1\n\t"
        "s_mov_b64 exec, -1\n\t"
        "s_cmp_ge_u32 s20, 4\n\t"
        "s_cbranch_scc0 L_g%=\n\t"
        "v_mov_b32 v102, 0x3fb8aa3b\n\t"
        "v_mul_f32 v102, s22, v102\n\t"
        "v_mul_f32 v96, -0.5, |v102|\n\t"
        "v_cvt_pk_bf16_f32 v199, v96, 0\n\t"
        "s_waitcnt vmcnt(5)\n\t"
        "v_dot2c_f32_bf16 v78, v112, v112\n\t"
        "v_dot2c_f32_bf16 v78, v113, v113\n\t"
        "v_dot2c_f32_bf16 v78, v114, v114\n\t"
        "v_dot2c_f32_bf16 v78, v115, v115\n\t"
        "v_dot2c_f32_bf16 v78, v116, v116\n\t"
        "v_dot2c_f32_bf16 v78, v117, v117\n\t"
        "v_dot2c_f32_bf16 v78, v118, v118\n\t"
        "s_nop 2\n\t"
        "v_mov_b32 v79, v78\n\t"
        "s_nop 1\n\t"
        "v_permlane32_swap_b32 v78, v79\n\t"
        "v_add_f32 v78, v78, v79\n\t"
        "v_sqrt_f32 v78, v78\n\t"
        "s_nop 0\n\t"
        "v_add_f32 v78, 0x358637bd, v78\n\t"
        "v_rcp_f32 v78, v78\n\t"
        "s_nop 0\n\t"
        "v_mul_f32 v78, v102, v78\n\t"
        "v_cvt_pk_bf16_f32 v96, v78, 0\n\t"
        "v_cvt_pk_bf16_f32 v97, 0, v78\n\t"
        "v_dot2c_f32_bf16 v64, v112, v96\n\t"
        "v_dot2c_f32_bf16 v65, v112, v97\n\t"
        "v_dot2c_f32_bf16 v66, v113, v96\n\t"
        "v_dot2c_f32_bf16 v67, v113, v97\n\t"
        "v_dot2c_f32_bf16 v68, v114, v96\n\t"
        "v_dot2c_f32_bf16 v69, v114, v97\n\t"
        "v_dot2c_f32_bf16 v70, v115, v96\n\t"
        "v_dot2c_f32_bf16 v71, v115, v97\n\t"
        "v_dot2c_f32_bf16 v72, v116, v96\n\t"
        "v_dot2c_f32_bf16 v73, v116, v97\n\t"
        "v_cvt_pk_bf16_f32 v192, v64, v65\n\t"
        "v_cvt_pk_bf16_f32 v193, v66, v67\n\t"
        "v_cvt_pk_bf16_f32 v194, v68, v69\n\t"
        "v_cvt_pk_bf16_f32 v195, v70, v71\n\t"
        "ds_write_b128 v107, v[192:195]\n\t"
        "v_dot2c_f32_bf16 v74, v117, v96\n\t"
        "v_dot2c_f32_bf16 v75, v117, v97\n\t"
        "v_dot2c_f32_bf16 v76, v118, v96\n\t"
        "v_dot2c_f32_bf16 v77, v118, v97\n\t"
        "v_cvt_pk_bf16_f32 v196, v72, v73\n\t"
        "s_nop 0\n\t"
        "v_cvt_pk_bf16_f32 v197, v74, v75\n\t"
        "v_cvt_pk_bf16_f32 v198, v76, v77\n\t"
        "ds_write_b128 v107, v[196:199] offset:1024\n\t"
        "ds_write_b32 v111, v108 offset:8164\n\t"
        "L_g%=:\n\t"
        "buffer_load_dwordx4 v3, s[44:47], s58 offen offset:0 lds\n\t"
        "buffer_load_dwordx4 v3, s[44:47], s58 offen offset:1024 lds\n\t"
        "s_waitcnt vmcnt(5)\n\t"
        "v_dot2c_f32_bf16 v30, v224, v224\n\t"
        "v_dot2c_f32_bf16 v30, v225, v225\n\t"
        "v_dot2c_f32_bf16 v30, v226, v226\n\t"
        "v_dot2c_f32_bf16 v30, v227, v227\n\t"
        "v_dot2c_f32_bf16 v30, v228, v228\n\t"
        "v_dot2c_f32_bf16 v30, v229, v229\n\t"
        "v_dot2c_f32_bf16 v30, v230, v230\n\t"
        "s_waitcnt vmcnt(3)\n\t"
        "v_dot2c_f32_bf16 v31, v232, v232\n\t"
        "v_dot2c_f32_bf16 v31, v233, v233\n\t"
        "v_dot2c_f32_bf16 v31, v234, v234\n\t"
        "v_dot2c_f32_bf16 v31, v235, v235\n\t"
        "v_dot2c_f32_bf16 v31, v236, v236\n\t"
        "v_dot2c_f32_bf16 v31, v237, v237\n\t"
        "v_dot2c_f32_bf16 v31, v238, v238\n\t"
        "buffer_load_dwordx4 v3, s[44:47], s58 offen offset:2048 lds\n\t"
        "s_mov_b32 exec_hi, 0\n\t"
        "buffer_load_dwordx4 v3, s[44:47], s58 offen offset:3072 lds\n\t"
        "s_mov_b32 exec_hi, -1\n\t"
        "v_permlane32_swap_b32 v30, v31\n\t"
        "v_add_f32 v30, v30, v31\n\t"
        "v_sqrt_f32 v30, v30\n\t"
        "s_nop 0\n\t"
        "v_add_f32 v30, 0x358637bd, v30\n\t"
        "v_rcp_f32 v30, v30\n\t"
        "s_nop 0\n\t"
        "v_cvt_pk_bf16_f32 v8, v30, 0\n\t"
        "v_cvt_pk_bf16_f32 v9, v30, 0\n\t"
        "v_nop\n\t"
        "v_nop\n\t"
        "v_permlane32_swap_b32 v8, v9\n\t"
        "v_lshlrev_b32 v46, 16, v8\n\t"
        "v_lshlrev_b32 v47, 16, v9\n\t"
        "v_dot2c_f32_bf16 v16, v224, v8\n\t"
        "v_dot2c_f32_bf16 v17, v224, v46\n\t"
        "v_dot2c_f32_bf16 v18, v225, v8\n\t"
        "v_dot2c_f32_bf16 v19, v225, v46\n\t"
        "v_dot2c_f32_bf16 v20, v226, v8\n\t"
        "v_dot2c_f32_bf16 v21, v226, v46\n\t"
        "v_dot2c_f32_bf16 v22, v227, v8\n\t"
        "v_dot2c_f32_bf16 v23, v227, v46\n\t"
        "v_dot2c_f32_bf16 v24, v228, v8\n\t"
        "v_dot2c_f32_bf16 v25, v228, v46\n\t"
        "v_dot2c_f32_bf16 v26, v229, v8\n\t"
        "v_dot2c_f32_bf16 v27, v229, v46\n\t"
        "v_dot2c_f32_bf16 v28, v230, v8\n\t"
        "v_dot2c_f32_bf16 v29, v230, v46\n\t"
        "v_dot2c_f32_bf16 v32, v232, v9\n\t"
        "v_dot2c_f32_bf16 v33, v232, v47\n\t"
        "v_cvt_pk_bf16_f32 v224, v16, v17\n\t"
        "v_dot2c_f32_bf16 v34, v233, v9\n\t"
        "v_dot2c_f32_bf16 v35, v233, v47\n\t"
        "v_cvt_pk_bf16_f32 v225, v18, v19\n\t"
        "v_dot2c_f32_bf16 v36, v234, v9\n\t"
        "v_dot2c_f32_bf16 v37, v234, v47\n\t"
        "v_cvt_pk_bf16_f32 v226, v20, v21\n\t"
        "v_dot2c_f32_bf16 v38, v235, v9\n\t"
        "v_dot2c_f32_bf16 v39, v235, v47\n\t"
        "v_cvt_pk_bf16_f32 v227, v22, v23\n\t"
        "v_dot2c_f32_bf16 v40, v236, v9\n\t"
        "v_dot2c_f32_bf16 v41, v236, v47\n\t"
        "v_cvt_pk_bf16_f32 v228, v24, v25\n\t"
        "v_dot2c_f32_bf16 v42, v237, v9\n\t"
        "v_dot2c_f32_bf16 v43, v237, v47\n\t"
        "v_cvt_pk_bf16_f32 v229, v26, v27\n\t"
        "v_dot2c_f32_bf16 v44, v238, v9\n\t"
        "v_dot2c_f32_bf16 v45, v238, v47\n\t"
        "v_cvt_pk_bf16_f32 v230, v28, v29\n\t"
        "v_cvt_pk_bf16_f32 v232, v32, v33\n\t"
        "v_cvt_pk_bf16_f32 v233, v34, v35\n\t"
        "v_cvt_pk_bf16_f32 v234, v36, v37\n\t"
        "v_cvt_pk_bf16_f32 v235, v38, v39\n\t"
        "v_cvt_pk_bf16_f32 v236, v40, v41\n\t"
        "v_cvt_pk_bf16_f32 v237, v42, v43\n\t"
        "v_cvt_pk_bf16_f32 v238, v44, v45\n\t"
        "ds_read_b32 v96, v110 offset:8164\n\t"
        "ds_read_b128 v[192:195], v104 offset:0\n\t"
        "ds_read_b128 v[196:199], v104 offset:1024\n\t"
        "s_cmp_eq_u32 s28, 0\n\t"
        "s_cbranch_scc1 L_pub%=\n\t"
        "L_pubd%=:\n\t"
        "s_waitcnt lgkmcnt(0)\n\t"
        "v_cmp_eq_u32 vcc, 0, v96\n\t"
        "s_nop 0\n\t"
        "s_cmp_eq_u64 vcc, 0\n\t"
        "s_cbranch_scc0 L_rq0%=\n\t"
        "L_gotq%=:\n\t"
        "s_setprio 2\n\t"
        "v_mfma_f32_32x32x16_bf16 v[64:79], v[224:227], v[192:195], 0\n\t"
        "ds_read_b128 v[200:203], v105 offset:0\n\t"
        "ds_read_b128 v[204:207], v105 offset:1024\n\t"
        "v_mfma_f32_32x32x16_bf16 v[64:79], v[228:231], v[196:199], v[64:79]\n\t"
        "ds_read_b128 v[208:211], v106 offset:0\n\t"
        "ds_read_b128 v[212:215], v106 offset:1024\n\t"
        "s_add_u32 s25, s18, 1\n\t"
        "s_and_b32 s25, s25, 3\n\t"
        "s_mul_i32 s59, s25, 3584\n\t"
        "s_lshl_b32 s25, s25, 12\n\t"
        "s_add_u32 s59, s59, s23\n\t"
        "s_add_u32 s19, s25, s31\n\t"
        "s_nop 1\n\t"
        "s_waitcnt lgkmcnt(2)\n\t"
        "v_mfma_f32_32x32x16_bf16 v[80:95], v[224:227], v[200:203], 0\n\t"
        "ds_read_b128 v[216:219], v107 offset:0\n\t"
        "ds_read_b128 v[220:223], v107 offset:1024\n\t"
        "v_exp_f32 v64, v64\n\t"
        "v_exp_f32 v65, v65\n\t"
        "v_exp_f32 v66, v66\n\t"
        "v_exp_f32 v67, v67\n\t"
        "v_mfma_f32_32x32x16_bf16 v[80:95], v[228:231], v[204:207], v[80:95]\n\t"
        "v_exp_f32 v68, v68\n\t"
        "v_exp_f32 v69, v69\n\t"
        "v_exp_f32 v70, v70\n\t"
        "v_exp_f32 v71, v71\n\t"
        "v_exp_f32 v72, v72\n\t"
        "v_exp_f32 v73, v73\n\t"
        "v_exp_f32 v74, v74\n\t"
        "v_exp_f32 v75, v75\n\t"
        "v_exp_f32 v76, v76\n\t"
        "v_exp_f32 v77, v77\n\t"
        "v_exp_f32 v78, v78\n\t"
        "v_exp_f32 v79, v79\n\t"
        "s_waitcnt lgkmcnt(2)\n\t"
        "v_mfma_f32_32x32x16_bf16 v[96:111], v[224:227], v[208:211], 0\n\t"
        "v_exp_f32 v80, v80\n\t"
        "v_exp_f32 v81, v81\n\t"
        "v_exp_f32 v82, v82\n\t"
        "v_exp_f32 v83, v83\n\t"
        "v_mfma_f32_32x32x16_bf16 v[96:111], v[228:231], v[212:215], v[96:111]\n\t"
        "s_cmp_eq_u32 s28, 0\n\t"
        "s_cbranch_scc1 L_flag%=\n\t"
        "L_flagd%=:\n\t"
        "s_waitcnt vmcnt(2)\n\t"
        "ds_read_b64_tr_b16 v[112:113], v5 offset:0\n\t"
        "ds_read_b64_tr_b16 v[114:115], v5 offset:224\n\t"
        "ds_read_b64_tr_b16 v[116:117], v5 offset:896\n\t"
        "ds_read_b64_tr_b16 v[118:119], v5 offset:1120\n\t"
        "v_exp_f32 v84, v84\n\t"
        "v_exp_f32 v85, v85\n\t"
        "v_exp_f32 v86, v86\n\t"
        "v_exp_f32 v87, v87\n\t"
        "v_exp_f32 v88, v88\n\t"
        "v_exp_f32 v89, v89\n\t"
        "v_exp_f32 v90, v90\n\t"
        "s_waitcnt lgkmcnt(2)\n\t"
        "v_smfmac_f32_32x32x32_bf16 v[128:143], v[112:115], v[64:71], v7\n\t"
        "v_exp_f32 v91, v91\n\t"
        "v_exp_f32 v92, v92\n\t"
        "v_exp_f32 v93, v93\n\t"
        "s_waitcnt lgkmcnt(0)\n\t"
        "v_smfmac_f32_32x32x32_bf16 v[128:143], v[116:119], v[72:79], v7\n\t"
        "v_exp_f32 v94, v94\n\t"
        "v_exp_f32 v95, v95\n\t"
        "v_mfma_f32_32x32x16_bf16 v[64:79], v[224:227], v[216:219], 0\n\t"
        "s_lshl_b32 s29, 1, s18\n\t"
        "s_mov_b32 s63, 12\n\t"
        "v_exp_f32 v96, v96\n\t"
        "v_exp_f32 v97, v97\n\t"
        "v_exp_f32 v98, v98\n\t"
        "v_mfma_f32_32x32x16_bf16 v[64:79], v[228:231], v[220:223], v[64:79]\n\t"
        "s_add_u32 s25, s18, 2\n\t"
        "s_and_b32 s25, s25, 3\n\t"
        "v_exp_f32 v99, v99\n\t"
        "v_exp_f32 v100, v100\n\t"
        "v_exp_f32 v101, v101\n\t"
        "v_smfmac_f32_32x32x32_bf16 v[144:159], v[112:115], v[80:87], v7\n\t"
        "v_exp_f32 v102, v102\n\t"
        "v_exp_f32 v103, v103\n\t"
        "v_exp_f32 v104, v104\n\t"
        "v_smfmac_f32_32x32x32_bf16 v[144:159], v[116:119], v[88:95], v7\n\t"
        "s_mul_i32 s60, s25, 3584\n\t"
        "s_lshl_b32 s25, s25, 12\n\t"
        "v_exp_f32 v105, v105\n\t"
        "v_exp_f32 v106, v106\n\t"
        "v_exp_f32 v107, v107\n\t"
        "v_smfmac_f32_32x32x32_bf16 v[160:175], v[112:115], v[96:103], v7\n\t"
        "s_add_u32 s60, s60, s23\n\t"
        "s_add_u32 s64, s25, s31\n\t"
        "s_add_u32 m0, s27, 4096\n\t"
        "v_exp_f32 v108, v108\n\t"
        "v_exp_f32 v109, v109\n\t"
        "v_exp_f32 v110, v110\n\t"
        "v_exp_f32 v111, v111\n\t"
        "v_mfma_f32_32x32x16_bf16 v[80:95], v[232:235], v[192:195], 0\n\t"
        "buffer_load_dwordx4 v3, s[44:47], s59 offen offset:0 lds\n\t"
        "v_exp_f32 v64, v64\n\t"
        "v_exp_f32 v65, v65\n\t"
        "v_exp_f32 v66, v66\n\t"
        "v_exp_f32 v67, v67\n\t"
        "v_mfma_f32_32x32x16_bf16 v[80:95], v[236:239], v[196:199], v[80:95]\n\t"
        "buffer_load_dwordx4 v3, s[44:47], s59 offen offset:1024 lds\n\t"
        "v_exp_f32 v68, v68\n\t"
        "v_exp_f32 v69, v69\n\t"
        "v_exp_f32 v70, v70\n\t"
        "v_exp_f32 v71, v71\n\t"
        "v_smfmac_f32_32x32x32_bf16 v[160:175], v[116:119], v[104:111], v7\n\t"
        "s_add_u32 s25, s18, 3\n\t"
        "s_and_b32 s25, s25, 3\n\t"
        "buffer_load_dwordx4 v3, s[44:47], s59 offen offset:2048 lds\n\t"
        "v_exp_f32 v72, v72\n\t"
        "v_exp_f32 v73, v73\n\t"
        "v_exp_f32 v74, v74\n\t"
        "v_exp_f32 v75, v75\n\t"
        "v_smfmac_f32_32x32x32_bf16 v[176:191], v[112:115], v[64:71], v7\n\t"
        "s_mov_b32 exec_hi, 0\n\t"
        "buffer_load_dwordx4 v3, s[44:47], s59 offen offset:3072 lds\n\t"
        "s_mov_b32 exec_hi, -1\n\t"
        "v_exp_f32 v76, v76\n\t"
        "v_exp_f32 v77, v77\n\t"
        "v_exp_f32 v78, v78\n\t"
        "v_exp_f32 v79, v79\n\t"
        "v_mfma_f32_32x32x16_bf16 v[96:111], v[232:235], v[200:203], 0\n\t"
        "s_mul_i32 s61, s25, 3584\n\t"
        "s_lshl_b32 s25, s25, 12\n\t"
        "s_waitcnt vmcnt(4)\n\t"
        "ds_read_b64_tr_b16 v[120:121], v5 offset:1792\n\t"
        "ds_read_b64_tr_b16 v[122:123], v5 offset:2016\n\t"
        "v_exp_f32 v80, v80\n\t"
        "v_exp_f32 v81, v81\n\t"
        "v_exp_f32 v82, v82\n\t"
        "v_exp_f32 v83, v83\n\t"
        "v_mfma_f32_32x32x16_bf16 v[96:111], v[236:239], v[204:207], v[96:111]\n\t"
        "s_add_u32 s61, s61, s23\n\t"
        "s_add_u32 s65, s25, s31\n\t"
        "ds_read_b64_tr_b16 v[124:125], v5 offset:2688\n\t"
        "ds_read_b64_tr_b16 v[126:127], v5 offset:2912\n\t"
        "v_exp_f32 v84, v84\n\t"
        "v_exp_f32 v85, v85\n\t"
        "v_exp_f32 v86, v86\n\t"
        "v_exp_f32 v87, v87\n\t"
        "v_smfmac_f32_32x32x32_bf16 v[176:191], v[116:119], v[72:79], v7\n\t"
        "v_exp_f32 v88, v88\n\t"
        "v_exp_f32 v89, v89\n\t"
        "v_exp_f32 v90, v90\n\t"
        "v_exp_f32 v91, v91\n\t"
        "s_waitcnt lgkmcnt(2)\n\t"
        "v_smfmac_f32_32x32x32_bf16 v[128:143], v[120:123], v[80:87], v7\n\t"
        "s_mov_b64 s[50:51], s[42:43]\n\t"
        "v_exp_f32 v92, v92\n\t"
        "v_exp_f32 v93, v93\n\t"
        "v_exp_f32 v94, v94\n\t"
        "v_exp_f32 v95, v95\n\t"
        "v_mfma_f32_32x32x16_bf16 v[64:79], v[232:235], v[208:211], 0\n\t"
        "v_exp_f32 v96, v96\n\t"
        "v_exp_f32 v97, v97\n\t"
        "v_exp_f32 v98, v98\n\t"
        "v_exp_f32 v99, v99\n\t"
        "v_mfma_f32_32x32x16_bf16 v[64:79], v[236:239], v[212:215], v[64:79]\n\t"
        "v_exp_f32 v100, v100\n\t"
        "v_exp_f32 v101, v101\n\t"
        "v_exp_f32 v102, v102\n\t"
        "v_exp_f32 v103, v103\n\t"
        "s_waitcnt lgkmcnt(0)\n\t"
        "v_smfmac_f32_32x32x32_bf16 v[128:143], v[124:127], v[88:95], v7\n\t"
        "s_mov_b64 exec, 15\n\t"
        "buffer_load_dword v1, v15, s[52:55], s30 offen sc1\n\t"
        "s_mov_b64 exec, -1\n\t"
        "v_exp_f32 v104, v104\n\t"
        "v_exp_f32 v105, v105\n\t"
        "v_exp_f32 v106, v106\n\t"
        "v_exp_f32 v107, v107\n\t"
        "v_smfmac_f32_32x32x32_bf16 v[144:159], v[120:123], v[96:103], v7\n\t"
        "v_exp_f32 v108, v108\n\t"
        "v_exp_f32 v109, v109\n\t"
        "v_exp_f32 v110, v110\n\t"
        "v_exp_f32 v111, v111\n\t"
        "s_waitcnt vmcnt(0)\n\t"
        "v_cmp_ne_u32 vcc, v1, v13\n\t"
        "v_mfma_f32_32x32x16_bf16 v[80:95], v[232:235], v[216:219], 0\n\t"
        "s_or_b32 s24, vcc_lo, s29\n\t"
        "s_and_b32 s24, s24, 15\n\t"
        "s_cmp_eq_u32 s24, 15\n\t"
        "s_cbranch_scc0 L_spin%=\n\t"
        "L_ready%=:\n\t"
        "buffer_load_dwordx4 v[240:243], v4, s[52:55], s19 offen offset:0 sc1\n\t"
        "buffer_load_dwordx4 v[244:247], v4, s[52:55], s19 offen offset:1024 sc1\n\t"
        "v_exp_f32 v64, v64\n\t"
        "v_exp_f32 v65, v65\n\t"
        "v_exp_f32 v66, v66\n\t"
        "v_exp_f32 v67, v67\n\t"
        "v_mfma_f32_32x32x16_bf16 v[80:95], v[236:239], v[220:223], v[80:95]\n\t"
        "v_exp_f32 v68, v68\n\t"
        "v_exp_f32 v69, v69\n\t"
        "v_exp_f32 v70, v70\n\t"
        "v_exp_f32 v71, v71\n\t"
        "v_smfmac_f32_32x32x32_bf16 v[144:159], v[124:127], v[104:111], v7\n\t"
        "buffer_load_dwordx4 v[248:251], v4, s[52:55], s19 offen offset:2048 sc1\n\t"
        "v_exp_f32 v72, v72\n\t"
        "v_exp_f32 v73, v73\n\t"
        "v_exp_f32 v74, v74\n\t"
        "v_exp_f32 v75, v75\n\t"
        "v_smfmac_f32_32x32x32_bf16 v[160:175], v[120:123], v[64:71], v7\n\t"
        "buffer_load_dwordx4 v[252:255], v4, s[52:55], s19 offen offset:3072 sc1\n\t"
        "v_exp_f32 v76, v76\n\t"
        "v_exp_f32 v77, v77\n\t"
        "v_exp_f32 v78, v78\n\t"
        "v_exp_f32 v79, v79\n\t"
        "v_exp_f32 v80, v80\n\t"
        "v_exp_f32 v81, v81\n\t"
        "v_smfmac_f32_32x32x32_bf16 v[160:175], v[124:127], v[72:79], v7\n\t"
        "v_exp_f32 v82, v82\n\t"
        "v_exp_f32 v83, v83\n\t"
        "v_exp_f32 v84, v84\n\t"
        "v_exp_f32 v85, v85\n\t"
        "v_exp_f32 v86, v86\n\t"
        "v_exp_f32 v87, v87\n\t"
        "s_waitcnt vmcnt(2)\n\t"
        "v_mfma_f32_32x32x16_bf16 v[96:111], v[240:243], v[192:195], 0\n\t"
        "ds_read_b64_tr_b16 v[16:17], v5 offset:4096\n\t"
        "ds_read_b64_tr_b16 v[18:19], v5 offset:4320\n\t"
        "v_exp_f32 v88, v88\n\t"
        "v_exp_f32 v89, v89\n\t"
        "v_exp_f32 v90, v90\n\t"
        "v_mfma_f32_32x32x16_bf16 v[96:111], v[244:247], v[196:199], v[96:111]\n\t"
        "ds_read_b64_tr_b16 v[20:21], v5 offset:4992\n\t"
        "ds_read_b64_tr_b16 v[22:23], v5 offset:5216\n\t"
        "v_exp_f32 v91, v91\n\t"
        "v_exp_f32 v92, v92\n\t"
        "v_smfmac_f32_32x32x32_bf16 v[176:191], v[120:123], v[80:87], v7\n\t"
        "s_add_u32 m0, s27, 8192\n\t"
        "v_exp_f32 v93, v93\n\t"
        "v_exp_f32 v94, v94\n\t"
        "v_exp_f32 v95, v95\n\t"
        "s_nop 0\n\t"
        "v_mfma_f32_32x32x16_bf16 v[64:79], v[240:243], v[200:203], 0\n\t"
        "buffer_load_dwordx4 v3, s[44:47], s60 offen offset:0 lds\n\t"
        "v_exp_f32 v96, v96\n\t"
        "v_exp_f32 v97, v97\n\t"
        "v_exp_f32 v98, v98\n\t"
        "v_exp_f32 v99, v99\n\t"
        "v_mfma_f32_32x32x16_bf16 v[64:79], v[244:247], v[204:207], v[64:79]\n\t"
        "buffer_load_dwordx4 v3, s[44:47], s60 offen offset:1024 lds\n\t"
        "v_exp_f32 v100, v100\n\t"
        "v_exp_f32 v101, v101\n\t"
        "v_exp_f32 v102, v102\n\t"
        "v_exp_f32 v103, v103\n\t"
        "v_smfmac_f32_32x32x32_bf16 v[176:191], v[124:127], v[88:95], v7\n\t"
        "buffer_load_dwordx4 v3, s[44:47], s60 offen offset:2048 lds\n\t"
        "s_waitcnt lgkmcnt(0)\n\t"
        "v_exp_f32 v104, v104\n\t"
        "v_exp_f32 v105, v105\n\t"
        "v_exp_f32 v106, v106\n\t"
        "v_exp_f32 v107, v107\n\t"
        "v_smfmac_f32_32x32x32_bf16 v[128:143], v[16:19], v[96:103], v7\n\t"
        "s_mov_b32 exec_hi, 0\n\t"
        "buffer_load_dwordx4 v3, s[44:47], s60 offen offset:3072 lds\n\t"
        "s_mov_b32 exec_hi, -1\n\t"
        "v_exp_f32 v108, v108\n\t"
        "v_exp_f32 v109, v109\n\t"
        "v_exp_f32 v110, v110\n\t"
        "v_exp_f32 v111, v111\n\t"
        "v_mfma_f32_32x32x16_bf16 v[80:95], v[240:243], v[208:211], 0\n\t"
        "ds_read_b64_tr_b16 v[24:25], v5 offset:5888\n\t"
        "ds_read_b64_tr_b16 v[26:27], v5 offset:6112\n\t"
        "buffer_load_dwordx4 v[48:51], v4, s[52:55], s64 offen offset:0 sc1\n\t"
        "v_exp_f32 v64, v64\n\t"
        "v_exp_f32 v65, v65\n\t"
        "v_exp_f32 v66, v66\n\t"
        "v_exp_f32 v67, v67\n\t"
        "v_mfma_f32_32x32x16_bf16 v[80:95], v[244:247], v[212:215], v[80:95]\n\t"
        "ds_read_b64_tr_b16 v[28:29], v5 offset:6784\n\t"
        "ds_read_b64_tr_b16 v[30:31], v5 offset:7008\n\t"
        "buffer_load_dwordx4 v[52:55], v4, s[52:55], s64 offen offset:1024 sc1\n\t"
        "v_exp_f32 v68, v68\n\t"
        "v_exp_f32 v69, v69\n\t"
        "v_exp_f32 v70, v70\n\t"
        "v_exp_f32 v71, v71\n\t"
        "v_smfmac_f32_32x32x32_bf16 v[128:143], v[20:23], v[104:111], v7\n\t"
        "buffer_load_dwordx4 v[56:59], v4, s[52:55], s64 offen offset:2048 sc1\n\t"
        "v_exp_f32 v72, v72\n\t"
        "v_exp_f32 v73, v73\n\t"
        "v_exp_f32 v74, v74\n\t"
        "v_exp_f32 v75, v75\n\t"
        "v_smfmac_f32_32x32x32_bf16 v[144:159], v[16:19], v[64:71], v7\n\t"
        "s_add_u32 m0, s27, 12288\n\t"
        "buffer_load_dwordx4 v[60:63], v4, s[52:55], s64 offen offset:3072 sc1\n\t"
        "s_waitcnt vmcnt(8)\n\t"
        "v_exp_f32 v76, v76\n\t"
        "v_exp_f32 v77, v77\n\t"
        "v_exp_f32 v78, v78\n\t"
        "v_exp_f32 v79, v79\n\t"
        "v_mfma_f32_32x32x16_bf16 v[96:111], v[248:251], v[192:195], 0\n\t"
        "buffer_load_dwordx4 v3, s[44:47], s61 offen offset:0 lds\n\t"
        "v_exp_f32 v80, v80\n\t"
        "v_exp_f32 v81, v81\n\t"
        "v_exp_f32 v82, v82\n\t"
        "v_exp_f32 v83, v83\n\t"
        "v_mfma_f32_32x32x16_bf16 v[96:111], v[252:255], v[196:199], v[96:111]\n\t"
        "buffer_load_dwordx4 v3, s[44:47], s61 offen offset:1024 lds\n\t"
        "v_exp_f32 v84, v84\n\t"
        "v_exp_f32 v85, v85\n\t"
        "v_exp_f32 v86, v86\n\t"
        "v_exp_f32 v87, v87\n\t"
        "v_smfmac_f32_32x32x32_bf16 v[144:159], v[20:23], v[72:79], v7\n\t"
        "buffer_load_dwordx4 v3, s[44:47], s61 offen offset:2048 lds\n\t"
        "s_waitcnt lgkmcnt(0)\n\t"
        "v_exp_f32 v88, v88\n\t"
        "v_exp_f32 v89, v89\n\t"
        "v_exp_f32 v90, v90\n\t"
        "v_exp_f32 v91, v91\n\t"
        "v_smfmac_f32_32x32x32_bf16 v[160:175], v[16:19], v[80:87], v7\n\t"
        "s_mov_b32 exec_hi, 0\n\t"
        "buffer_load_dwordx4 v3, s[44:47], s61 offen offset:3072 lds\n\t"
        "s_mov_b32 exec_hi, -1\n\t"
        "v_exp_f32 v92, v92\n\t"
        "v_exp_f32 v93, v93\n\t"
        "v_exp_f32 v94, v94\n\t"
        "v_exp_f32 v95, v95\n\t"
        "v_mfma_f32_32x32x16_bf16 v[64:79], v[248:251], v[200:203], 0\n\t"
        "buffer_load_dwordx4 v[224:227], v4, s[52:55], s65 offen offset:0 sc1\n\t"
        "v_exp_f32 v96, v96\n\t"
        "v_exp_f32 v97, v97\n\t"
        "v_exp_f32 v98, v98\n\t"
        "v_exp_f32 v99, v99\n\t"
        "v_mfma_f32_32x32x16_bf16 v[64:79], v[252:255], v[204:207], v[64:79]\n\t"
        "buffer_load_dwordx4 v[228:231], v4, s[52:55], s65 offen offset:1024 sc1\n\t"
        "v_exp_f32 v100, v100\n\t"
        "v_exp_f32 v101, v101\n\t"
        "v_exp_f32 v102, v102\n\t"
        "v_exp_f32 v103, v103\n\t"
        "v_smfmac_f32_32x32x32_bf16 v[160:175], v[20:23], v[88:95], v7\n\t"
        "buffer_load_dwordx4 v[232:235], v4, s[52:55], s65 offen offset:2048 sc1\n\t"
        "v_exp_f32 v104, v104\n\t"
        "v_exp_f32 v105, v105\n\t"
        "v_exp_f32 v106, v106\n\t"
        "v_exp_f32 v107, v107\n\t"
        "v_smfmac_f32_32x32x32_bf16 v[128:143], v[24:27], v[96:103], v7\n\t"
        "buffer_load_dwordx4 v[236:239], v4, s[52:55], s65 offen offset:3072 sc1\n\t"
        "v_exp_f32 v108, v108\n\t"
        "v_exp_f32 v109, v109\n\t"
        "v_exp_f32 v110, v110\n\t"
        "v_exp_f32 v111, v111\n\t"
        "v_mfma_f32_32x32x16_bf16 v[80:95], v[248:251], v[208:211], 0\n\t"
        "s_waitcnt vmcnt(14)\n\t"
        "ds_read_b64_tr_b16 v[32:33], v6 offset:0\n\t"
        "ds_read_b64_tr_b16 v[34:35], v6 offset:224\n\t"
        "v_exp_f32 v64, v64\n\t"
        "v_exp_f32 v65, v65\n\t"
        "v_exp_f32 v66, v66\n\t"
        "v_exp_f32 v67, v67\n\t"
        "v_mfma_f32_32x32x16_bf16 v[80:95], v[252:255], v[212:215], v[80:95]\n\t"
        "ds_read_b64_tr_b16 v[36:37], v6 offset:896\n\t"
        "ds_read_b64_tr_b16 v[38:39], v6 offset:1120\n\t"
        "v_exp_f32 v68, v68\n\t"
        "v_exp_f32 v69, v69\n\t"
        "v_exp_f32 v70, v70\n\t"
        "v_exp_f32 v71, v71\n\t"
        "v_smfmac_f32_32x32x32_bf16 v[128:143], v[28:31], v[104:111], v7\n\t"
        "v_exp_f32 v72, v72\n\t"
        "v_exp_f32 v73, v73\n\t"
        "v_exp_f32 v74, v74\n\t"
        "v_exp_f32 v75, v75\n\t"
        "v_smfmac_f32_32x32x32_bf16 v[144:159], v[24:27], v[64:71], v7\n\t"
        "s_waitcnt vmcnt(10)\n\t"
        "v_exp_f32 v76, v76\n\t"
        "v_exp_f32 v77, v77\n\t"
        "v_exp_f32 v78, v78\n\t"
        "v_exp_f32 v79, v79\n\t"
        "v_mfma_f32_32x32x16_bf16 v[96:111], v[48:51], v[192:195], 0\n\t"
        "ds_read_b64_tr_b16 v[40:41], v6 offset:1792\n\t"
        "ds_read_b64_tr_b16 v[42:43], v6 offset:2016\n\t"
        "v_exp_f32 v80, v80\n\t"
        "v_exp_f32 v81, v81\n\t"
        "v_exp_f32 v82, v82\n\t"
        "v_exp_f32 v83, v83\n\t"
        "v_mfma_f32_32x32x16_bf16 v[96:111], v[52:55], v[196:199], v[96:111]\n\t"
        "ds_read_b64_tr_b16 v[44:45], v6 offset:2688\n\t"
        "ds_read_b64_tr_b16 v[46:47], v6 offset:2912\n\t"
        "v_exp_f32 v84, v84\n\t"
        "v_exp_f32 v85, v85\n\t"
        "v_exp_f32 v86, v86\n\t"
        "v_exp_f32 v87, v87\n\t"
        "v_smfmac_f32_32x32x32_bf16 v[144:159], v[28:31], v[72:79], v7\n\t"
        "s_waitcnt lgkmcnt(4)\n\t"
        "v_exp_f32 v88, v88\n\t"
        "v_exp_f32 v89, v89\n\t"
        "v_exp_f32 v90, v90\n\t"
        "v_exp_f32 v91, v91\n\t"
        "v_smfmac_f32_32x32x32_bf16 v[160:175], v[24:27], v[80:87], v7\n\t"
        "s_waitcnt vmcnt(8)\n\t"
        "v_exp_f32 v92, v92\n\t"
        "v_exp_f32 v93, v93\n\t"
        "v_exp_f32 v94, v94\n\t"
        "v_exp_f32 v95, v95\n\t"
        "v_mfma_f32_32x32x16_bf16 v[64:79], v[56:59], v[192:195], 0\n\t"
        "s_waitcnt vmcnt(6)\n\t"
        "ds_read_b64_tr_b16 v[112:113], v6 offset:4096\n\t"
        "ds_read_b64_tr_b16 v[114:115], v6 offset:4320\n\t"
        "s_setprio 1\n\t"
        "v_exp_f32 v96, v96\n\t"
        "v_exp_f32 v97, v97\n\t"
        "v_exp_f32 v98, v98\n\t"
        "v_exp_f32 v99, v99\n\t"
        "v_mfma_f32_32x32x16_bf16 v[64:79], v[60:63], v[196:199], v[64:79]\n\t"
        "ds_read_b64_tr_b16 v[116:117], v6 offset:4992\n\t"
        "ds_read_b64_tr_b16 v[118:119], v6 offset:5216\n\t"
        "v_exp_f32 v100, v100\n\t"
        "v_exp_f32 v101, v101\n\t"
        "v_exp_f32 v102, v102\n\t"
        "v_exp_f32 v103, v103\n\t"
        "v_smfmac_f32_32x32x32_bf16 v[160:175], v[28:31], v[88:95], v7\n\t"
        "s_waitcnt lgkmcnt(4)\n\t"
        "v_exp_f32 v104, v104\n\t"
        "v_exp_f32 v105, v105\n\t"
        "v_exp_f32 v106, v106\n\t"
        "v_exp_f32 v107, v107\n\t"
        "v_smfmac_f32_32x32x32_bf16 v[128:143], v[32:35], v[96:103], v7\n\t"
        "s_waitcnt vmcnt(2)\n\t"
        "v_exp_f32 v108, v108\n\t"
        "v_exp_f32 v109, v109\n\t"
        "v_exp_f32 v110, v110\n\t"
        "v_exp_f32 v111, v111\n\t"
        "v_mfma_f32_32x32x16_bf16 v[80:95], v[224:227], v[192:195], 0\n\t"
        "ds_read_b64_tr_b16 v[120:121], v6 offset:5888\n\t"
        "ds_read_b64_tr_b16 v[122:123], v6 offset:6112\n\t"
        "v_exp_f32 v64, v64\n\t"
        "v_exp_f32 v65, v65\n\t"
        "v_exp_f32 v66, v66\n\t"
        "v_exp_f32 v67, v67\n\t"
        "v_mfma_f32_32x32x16_bf16 v[80:95], v[228:231], v[196:199], v[80:95]\n\t"
        "ds_read_b64_tr_b16 v[124:125], v6 offset:6784\n\t"
        "ds_read_b64_tr_b16 v[126:127], v6 offset:7008\n\t"
        "v_exp_f32 v68, v68\n\t"
        "v_exp_f32 v69, v69\n\t"
        "v_exp_f32 v70, v70\n\t"
        "v_exp_f32 v71, v71\n\t"
        "v_smfmac_f32_32x32x32_bf16 v[128:143], v[36:39], v[104:111], v7\n\t"
        "s_waitcnt lgkmcnt(4)\n\t"
        "v_exp_f32 v72, v72\n\t"
        "v_exp_f32 v73, v73\n\t"
        "v_exp_f32 v74, v74\n\t"
        "v_exp_f32 v75, v75\n\t"
        "v_smfmac_f32_32x32x32_bf16 v[128:143], v[40:43], v[64:71], v7\n\t"
        "s_waitcnt vmcnt(0)\n\t"
        "v_exp_f32 v76, v76\n\t"
        "v_exp_f32 v77, v77\n\t"
        "v_exp_f32 v78, v78\n\t"
        "v_exp_f32 v79, v79\n\t"
        "v_mfma_f32_32x32x16_bf16 v[96:111], v[232:235], v[192:195], 0\n\t"
        "v_exp_f32 v80, v80\n\t"
        "v_exp_f32 v81, v81\n\t"
        "v_exp_f32 v82, v82\n\t"
        "v_exp_f32 v83, v83\n\t"
        "v_mfma_f32_32x32x16_bf16 v[96:111], v[236:239], v[196:199], v[96:111]\n\t"
        "v_exp_f32 v84, v84\n\t"
        "v_exp_f32 v85, v85\n\t"
        "v_exp_f32 v86, v86\n\t"
        "v_exp_f32 v87, v87\n\t"
        "v_smfmac_f32_32x32x32_bf16 v[128:143], v[44:47], v[72:79], v7\n\t"
        "s_waitcnt lgkmcnt(0)\n\t"
        "v_exp_f32 v88, v88\n\t"
        "v_exp_f32 v89, v89\n\t"
        "v_exp_f32 v90, v90\n\t"
        "v_exp_f32 v91, v91\n\t"
        "v_smfmac_f32_32x32x32_bf16 v[128:143], v[112:115], v[80:87], v7\n\t"
        "v_exp_f32 v92, v92\n\t"
        "v_exp_f32 v93, v93\n\t"
        "v_exp_f32 v94, v94\n\t"
        "v_exp_f32 v95, v95\n\t"
        "v_mfma_f32_32x32x16_bf16 v[64:79], v[48:51], v[200:203], 0\n\t"
        "v_exp_f32 v96, v96\n\t"
        "v_exp_f32 v97, v97\n\t"
        "v_exp_f32 v98, v98\n\t"
        "v_exp_f32 v99, v99\n\t"
        "v_mfma_f32_32x32x16_bf16 v[64:79], v[52:55], v[204:207], v[64:79]\n\t"
        "v_exp_f32 v100, v100\n\t"
        "v_exp_f32 v101, v101\n\t"
        "v_exp_f32 v102, v102\n\t"
        "v_exp_f32 v103, v103\n\t"
        "v_smfmac_f32_32x32x32_bf16 v[128:143], v[116:119], v[88:95], v7\n\t"
        "v_exp_f32 v104, v104\n\t"
        "v_exp_f32 v105, v105\n\t"
        "v_exp_f32 v106, v106\n\t"
        "v_exp_f32 v107, v107\n\t"
        "v_smfmac_f32_32x32x32_bf16 v[128:143], v[120:123], v[96:103], v7\n\t"
        "v_exp_f32 v108, v108\n\t"
        "v_exp_f32 v109, v109\n\t"
        "v_exp_f32 v110, v110\n\t"
        "v_exp_f32 v111, v111\n\t"
        "v_mfma_f32_32x32x16_bf16 v[80:95], v[56:59], v[200:203], 0\n\t"
        "v_exp_f32 v64, v64\n\t"
        "v_exp_f32 v65, v65\n\t"
        "v_exp_f32 v66, v66\n\t"
        "v_smfmac_f32_32x32x32_bf16 v[128:143], v[124:127], v[104:111], v7\n\t"
        "v_exp_f32 v67, v67\n\t"
        "v_exp_f32 v68, v68\n\t"
        "v_exp_f32 v69, v69\n\t"
        "v_mfma_f32_32x32x16_bf16 v[80:95], v[60:63], v[204:207], v[80:95]\n\t"
        "v_exp_f32 v70, v70\n\t"
        "v_exp_f32 v71, v71\n\t"
        "v_exp_f32 v72, v72\n\t"
        "v_exp_f32 v73, v73\n\t"
        "v_smfmac_f32_32x32x32_bf16 v[144:159], v[32:35], v[64:71], v7\n\t"
        "v_exp_f32 v74, v74\n\t"
        "v_exp_f32 v75, v75\n\t"
        "v_exp_f32 v76, v76\n\t"
        "v_exp_f32 v77, v77\n\t"
        "ds_write_b128 v4, v[128:131] offset:0\n\t"
        "v_exp_f32 v78, v78\n\t"
        "v_exp_f32 v79, v79\n\t"
        "v_mfma_f32_32x32x16_bf16 v[96:111], v[224:227], v[200:203], 0\n\t"
        "v_exp_f32 v80, v80\n\t"
        "v_exp_f32 v81, v81\n\t"
        "v_exp_f32 v82, v82\n\t"
        "v_exp_f32 v83, v83\n\t"
        "v_mfma_f32_32x32x16_bf16 v[96:111], v[228:231], v[204:207], v[96:111]\n\t"
        "v_exp_f32 v84, v84\n\t"
        "v_exp_f32 v85, v85\n\t"
        "v_exp_f32 v86, v86\n\t"
        "v_exp_f32 v87, v87\n\t"
        "v_smfmac_f32_32x32x32_bf16 v[144:159], v[36:39], v[72:79], v7\n\t"
        "v_exp_f32 v88, v88\n\t"
        "v_exp_f32 v89, v89\n\t"
        "ds_write_b128 v4, v[132:135] offset:1024\n\t"
        "v_exp_f32 v90, v90\n\t"
        "v_exp_f32 v91, v91\n\t"
        "v_smfmac_f32_32x32x32_bf16 v[144:159], v[40:43], v[80:87], v7\n\t"
        "v_exp_f32 v92, v92\n\t"
        "v_exp_f32 v93, v93\n\t"
        "v_exp_f32 v94, v94\n\t"
        "v_exp_f32 v95, v95\n\t"
        "v_mfma_f32_32x32x16_bf16 v[64:79], v[232:235], v[200:203], 0\n\t"
        "v_exp_f32 v96, v96\n\t"
        "v_exp_f32 v97, v97\n\t"
        "v_exp_f32 v98, v98\n\t"
        "v_exp_f32 v99, v99\n\t"
        "v_mfma_f32_32x32x16_bf16 v[64:79], v[236:239], v[204:207], v[64:79]\n\t"
        "v_exp_f32 v100, v100\n\t"
        "v_exp_f32 v101, v101\n\t"
        "ds_write_b128 v4, v[136:139] offset:2048\n\t"
        "v_exp_f32 v102, v102\n\t"
        "v_exp_f32 v103, v103\n\t"
        "v_smfmac_f32_32x32x32_bf16 v[144:159], v[44:47], v[88:95], v7\n\t"
        "v_exp_f32 v104, v104\n\t"
        "v_exp_f32 v105, v105\n\t"
        "v_exp_f32 v106, v106\n\t"
        "v_exp_f32 v107, v107\n\t"
        "v_smfmac_f32_32x32x32_bf16 v[144:159], v[112:115], v[96:103], v7\n\t"
        "v_exp_f32 v108, v108\n\t"
        "v_exp_f32 v109, v109\n\t"
        "v_exp_f32 v110, v110\n\t"
        "v_exp_f32 v111, v111\n\t"
        "v_mfma_f32_32x32x16_bf16 v[80:95], v[48:51], v[208:211], 0\n\t"
        "v_exp_f32 v64, v64\n\t"
        "v_exp_f32 v65, v65\n\t"
        "ds_write_b128 v4, v[140:143] offset:3072\n\t"
        "v_exp_f32 v66, v66\n\t"
        "v_exp_f32 v67, v67\n\t"
        "v_mfma_f32_32x32x16_bf16 v[80:95], v[52:55], v[212:215], v[80:95]\n\t"
        "v_exp_f32 v68, v68\n\t"
        "v_exp_f32 v69, v69\n\t"
        "v_exp_f32 v70, v70\n\t"
        "v_exp_f32 v71, v71\n\t"
        "v_smfmac_f32_32x32x32_bf16 v[144:159], v[116:119], v[104:111], v7\n\t"
        "v_exp_f32 v72, v72\n\t"
        "v_exp_f32 v73, v73\n\t"
        "v_exp_f32 v74, v74\n\t"
        "v_exp_f32 v75, v75\n\t"
        "v_smfmac_f32_32x32x32_bf16 v[144:159], v[120:123], v[64:71], v7\n\t"
        "v_exp_f32 v76, v76\n\t"
        "v_exp_f32 v77, v77\n\t"
        "v_exp_f32 v78, v78\n\t"
        "v_exp_f32 v79, v79\n\t"
        "v_mfma_f32_32x32x16_bf16 v[96:111], v[56:59], v[208:211], 0\n\t"
        "v_exp_f32 v80, v80\n\t"
        "v_exp_f32 v81, v81\n\t"
        "v_exp_f32 v82, v82\n\t"
        "v_smfmac_f32_32x32x32_bf16 v[144:159], v[124:127], v[72:79], v7\n\t"
        "v_exp_f32 v83, v83\n\t"
        "v_exp_f32 v84, v84\n\t"
        "v_exp_f32 v85, v85\n\t"
        "v_mfma_f32_32x32x16_bf16 v[96:111], v[60:63], v[212:215], v[96:111]\n\t"
        "v_exp_f32 v86, v86\n\t"
        "v_exp_f32 v87, v87\n\t"
        "v_exp_f32 v88, v88\n\t"
        "v_exp_f32 v89, v89\n\t"
        "v_smfmac_f32_32x32x32_bf16 v[160:175], v[32:35], v[80:87], v7\n\t"
        "v_exp_f32 v90, v90\n\t"
        "v_exp_f32 v91, v91\n\t"
        "v_exp_f32 v92, v92\n\t"
        "v_exp_f32 v93, v93\n\t"
        "ds_write_b128 v4, v[144:147] offset:4096\n\t"
        "v_exp_f32 v94, v94\n\t"
        "v_exp_f32 v95, v95\n\t"
        "v_mfma_f32_32x32x16_bf16 v[64:79], v[224:227], v[208:211], 0\n\t"
        "ds_read_b32 v13, v10 offset:3072\n\t"
        "v_exp_f32 v96, v96\n\t"
        "v_exp_f32 v97, v97\n\t"
        "v_exp_f32 v98, v98\n\t"
        "v_exp_f32 v99, v99\n\t"
        "v_mfma_f32_32x32x16_bf16 v[64:79], v[228:231], v[212:215], v[64:79]\n\t"
        "ds_read_b128 v[128:131], v10 offset:0\n\t"
        "v_exp_f32 v100, v100\n\t"
        "v_exp_f32 v101, v101\n\t"
        "v_exp_f32 v102, v102\n\t"
        "v_exp_f32 v103, v103\n\t"
        "v_smfmac_f32_32x32x32_bf16 v[160:175], v[36:39], v[88:95], v7\n\t"
        "ds_read_b128 v[132:135], v10 offset:1024\n\t"
        "v_exp_f32 v104, v104\n\t"
        "v_exp_f32 v105, v105\n\t"
        "ds_write_b128 v4, v[148:151] offset:5120\n\t"
        "v_exp_f32 v106, v106\n\t"
        "v_exp_f32 v107, v107\n\t"
        "v_smfmac_f32_32x32x32_bf16 v[160:175], v[40:43], v[96:103], v7\n\t"
        "ds_read_b128 v[136:139], v10 offset:2048\n\t"
        "v_exp_f32 v108, v108\n\t"
        "v_exp_f32 v109, v109\n\t"
        "v_exp_f32 v110, v110\n\t"
        "v_exp_f32 v111, v111\n\t"
        "v_mfma_f32_32x32x16_bf16 v[80:95], v[232:235], v[208:211], 0\n\t"
        "ds_read_b128 v[140:143], v10 offset:3072\n\t"
        "v_exp_f32 v64, v64\n\t"
        "v_exp_f32 v65, v65\n\t"
        "v_exp_f32 v66, v66\n\t"
        "v_exp_f32 v67, v67\n\t"
        "v_mfma_f32_32x32x16_bf16 v[80:95], v[236:239], v[212:215], v[80:95]\n\t"
        "v_exp_f32 v68, v68\n\t"
        "v_exp_f32 v69, v69\n\t"
        "ds_write_b128 v4, v[152:155] offset:6144\n\t"
        "v_exp_f32 v70, v70\n\t"
        "v_exp_f32 v71, v71\n\t"
        "s_waitcnt lgkmcnt(1)\n\t"
        "v_readlane_b32 s24, v13, 63\n\t"
        "v_smfmac_f32_32x32x32_bf16 v[160:175], v[44:47], v[104:111], v7\n\t"
        "s_cmp_lg_u32 s24, 0\n\t"
        "s_cbranch_scc0 L_rx0%=\n\t"
        "L_got0%=:\n\t"
        "s_setprio 3\n\t"
        "v_exp_f32 v72, v72\n\t"
        "v_exp_f32 v73, v73\n\t"
        "v_exp_f32 v74, v74\n\t"
        "v_exp_f32 v75, v75\n\t"
        "v_smfmac_f32_32x32x32_bf16 v[160:175], v[112:115], v[64:71], v7\n\t"
        "v_exp_f32 v76, v76\n\t"
        "v_exp_f32 v77, v77\n\t"
        "v_exp_f32 v78, v78\n\t"
        "v_exp_f32 v79, v79\n\t"
        "v_pk_add_f32 v[176:177], v[176:177], v[128:129]\n\t"
        "v_pk_add_f32 v[178:179], v[178:179], v[130:131]\n\t"
        "v_pk_add_f32 v[180:181], v[180:181], v[132:133]\n\t"
        "v_pk_add_f32 v[182:183], v[182:183], v[134:135]\n\t"
        "v_pk_add_f32 v[184:185], v[184:185], v[136:137]\n\t"
        "v_pk_add_f32 v[186:187], v[186:187], v[138:139]\n\t"
        "v_pk_add_f32 v[188:189], v[188:189], v[140:141]\n\t"
        "v_pk_add_f32 v[190:191], v[190:191], v[142:143]\n\t"
        "s_setprio 1\n\t"
        "v_mfma_f32_32x32x16_bf16 v[96:111], v[240:243], v[216:219], 0\n\t"
        "v_exp_f32 v80, v80\n\t"
        "v_exp_f32 v81, v81\n\t"
        "ds_write_b128 v4, v[156:159] offset:7168\n\t"
        "v_exp_f32 v82, v82\n\t"
        "v_exp_f32 v83, v83\n\t"
        "v_mfma_f32_32x32x16_bf16 v[96:111], v[244:247], v[220:223], v[96:111]\n\t"
        "v_exp_f32 v84, v84\n\t"
        "v_exp_f32 v85, v85\n\t"
        "v_exp_f32 v86, v86\n\t"
        "v_exp_f32 v87, v87\n\t"
        "v_smfmac_f32_32x32x32_bf16 v[160:175], v[116:119], v[72:79], v7\n\t"
        "v_exp_f32 v88, v88\n\t"
        "v_exp_f32 v89, v89\n\t"
        "v_exp_f32 v90, v90\n\t"
        "v_exp_f32 v91, v91\n\t"
        "v_smfmac_f32_32x32x32_bf16 v[160:175], v[120:123], v[80:87], v7\n\t"
        "v_exp_f32 v92, v92\n\t"
        "v_exp_f32 v93, v93\n\t"
        "v_exp_f32 v94, v94\n\t"
        "v_exp_f32 v95, v95\n\t"
        "v_mfma_f32_32x32x16_bf16 v[64:79], v[248:251], v[216:219], 0\n\t"
        "v_exp_f32 v96, v96\n\t"
        "v_exp_f32 v97, v97\n\t"
        "v_exp_f32 v98, v98\n\t"
        "v_smfmac_f32_32x32x32_bf16 v[160:175], v[124:127], v[88:95], v7\n\t"
        "v_exp_f32 v99, v99\n\t"
        "v_exp_f32 v100, v100\n\t"
        "v_exp_f32 v101, v101\n\t"
        "v_mfma_f32_32x32x16_bf16 v[64:79], v[252:255], v[220:223], v[64:79]\n\t"
        "v_exp_f32 v102, v102\n\t"
        "v_exp_f32 v103, v103\n\t"
        "v_exp_f32 v104, v104\n\t"
        "v_exp_f32 v105, v105\n\t"
        "v_smfmac_f32_32x32x32_bf16 v[176:191], v[16:19], v[96:103], v7\n\t"
        "v_exp_f32 v106, v106\n\t"
        "v_exp_f32 v107, v107\n\t"
        "v_exp_f32 v108, v108\n\t"
        "v_exp_f32 v109, v109\n\t"
        "ds_write_b128 v4, v[160:163] offset:8192\n\t"
        "v_exp_f32 v110, v110\n\t"
        "v_exp_f32 v111, v111\n\t"
        "v_mfma_f32_32x32x16_bf16 v[80:95], v[48:51], v[216:219], 0\n\t"
        "ds_read_b32 v13, v11 offset:7168\n\t"
        "v_exp_f32 v64, v64\n\t"
        "v_exp_f32 v65, v65\n\t"
        "v_exp_f32 v66, v66\n\t"
        "v_exp_f32 v67, v67\n\t"
        "v_mfma_f32_32x32x16_bf16 v[80:95], v[52:55], v[220:223], v[80:95]\n\t"
        "ds_read_b128 v[128:131], v11 offset:4096\n\t"
        "v_exp_f32 v68, v68\n\t"
        "v_exp_f32 v69, v69\n\t"
        "ds_write_b128 v4, v[164:167] offset:9216\n\t"
        "v_exp_f32 v70, v70\n\t"
        "v_exp_f32 v71, v71\n\t"
        "v_smfmac_f32_32x32x32_bf16 v[176:191], v[20:23], v[104:111], v7\n\t"
        "ds_read_b128 v[132:135], v11 offset:5120\n\t"
        "v_exp_f32 v72, v72\n\t"
        "v_exp_f32 v73, v73\n\t"
        "v_exp_f32 v74, v74\n\t"
        "v_exp_f32 v75, v75\n\t"
        "v_smfmac_f32_32x32x32_bf16 v[176:191], v[24:27], v[64:71], v7\n\t"
        "ds_read_b128 v[136:139], v11 offset:6144\n\t"
        "v_exp_f32 v76, v76\n\t"
        "v_exp_f32 v77, v77\n\t"
        "ds_write_b128 v4, v[168:171] offset:10240\n\t"
        "v_exp_f32 v78, v78\n\t"
        "v_exp_f32 v79, v79\n\t"
        "v_mfma_f32_32x32x16_bf16 v[96:111], v[56:59], v[216:219], 0\n\t"
        "s_setprio 0\n\t"
        "ds_read_b128 v[140:143], v11 offset:7168\n\t"
        "v_exp_f32 v80, v80\n\t"
        "v_exp_f32 v81, v81\n\t"
        "v_exp_f32 v82, v82\n\t"
        "v_exp_f32 v83, v83\n\t"
        "v_mfma_f32_32x32x16_bf16 v[96:111], v[60:63], v[220:223], v[96:111]\n\t"
        "v_exp_f32 v84, v84\n\t"
        "v_exp_f32 v85, v85\n\t"
        "ds_write_b128 v4, v[172:175] offset:11264\n\t"
        "v_exp_f32 v86, v86\n\t"
        "v_exp_f32 v87, v87\n\t"
        "s_waitcnt lgkmcnt(1)\n\t"
        "v_readlane_b32 s24, v13, 63\n\t"
        "v_smfmac_f32_32x32x32_bf16 v[176:191], v[28:31], v[72:79], v7\n\t"
        "s_cmp_lg_u32 s24, 0\n\t"
        "s_cbranch_scc0 L_rx1%=\n\t"
        "L_got1%=:\n\t"
        "v_exp_f32 v88, v88\n\t"
        "v_exp_f32 v89, v89\n\t"
        "v_exp_f32 v90, v90\n\t"
        "v_exp_f32 v91, v91\n\t"
        "v_smfmac_f32_32x32x32_bf16 v[176:191], v[32:35], v[80:87], v7\n\t"
        "v_exp_f32 v92, v92\n\t"
        "v_exp_f32 v93, v93\n\t"
        "v_exp_f32 v94, v94\n\t"
        "v_exp_f32 v95, v95\n\t"
        "v_mfma_f32_32x32x16_bf16 v[64:79], v[224:227], v[216:219], 0\n\t"
        "v_exp_f32 v96, v96\n\t"
        "v_exp_f32 v97, v97\n\t"
        "v_exp_f32 v98, v98\n\t"
        "v_exp_f32 v99, v99\n\t"
        "v_mfma_f32_32x32x16_bf16 v[64:79], v[228:231], v[220:223], v[64:79]\n\t"
        "v_exp_f32 v100, v100\n\t"
        "v_exp_f32 v101, v101\n\t"
        "v_exp_f32 v102, v102\n\t"
        "v_exp_f32 v103, v103\n\t"
        "v_smfmac_f32_32x32x32_bf16 v[176:191], v[36:39], v[88:95], v7\n\t"
        "v_exp_f32 v104, v104\n\t"
        "v_exp_f32 v105, v105\n\t"
        "v_exp_f32 v106, v106\n\t"
        "v_exp_f32 v107, v107\n\t"
        "v_smfmac_f32_32x32x32_bf16 v[176:191], v[40:43], v[96:103], v7\n\t"
        "ds_read_b32 v15, v4 offset:31744\n\t"
        "ds_read_b128 v[160:163], v4 offset:28672\n\t"
        "v_exp_f32 v108, v108\n\t"
        "v_exp_f32 v109, v109\n\t"
        "v_exp_f32 v110, v110\n\t"
        "v_exp_f32 v111, v111\n\t"
        "v_mfma_f32_32x32x16_bf16 v[80:95], v[232:235], v[216:219], 0\n\t"
        "ds_read_b128 v[164:167], v4 offset:29696\n\t"
        "v_exp_f32 v64, v64\n\t"
        "v_exp_f32 v65, v65\n\t"
        "v_exp_f32 v66, v66\n\t"
        "v_exp_f32 v67, v67\n\t"
        "v_mfma_f32_32x32x16_bf16 v[80:95], v[236:239], v[220:223], v[80:95]\n\t"
        "ds_read_b128 v[168:171], v4 offset:30720\n\t"
        "ds_read_b128 v[172:175], v4 offset:31744\n\t"
        "v_exp_f32 v68, v68\n\t"
        "v_exp_f32 v69, v69\n\t"
        "v_exp_f32 v70, v70\n\t"
        "v_exp_f32 v71, v71\n\t"
        "v_smfmac_f32_32x32x32_bf16 v[176:191], v[44:47], v[104:111], v7\n\t"
        "ds_read_b32 v13, v12 offset:11264\n\t"
        "ds_read_b128 v[144:147], v12 offset:8192\n\t"
        "v_exp_f32 v72, v72\n\t"
        "v_exp_f32 v73, v73\n\t"
        "v_exp_f32 v74, v74\n\t"
        "v_exp_f32 v75, v75\n\t"
        "v_smfmac_f32_32x32x32_bf16 v[176:191], v[112:115], v[64:71], v7\n\t"
        "ds_read_b128 v[148:151], v12 offset:9216\n\t"
        "ds_read_b128 v[152:155], v12 offset:10240\n\t"
        "v_exp_f32 v76, v76\n\t"
        "v_exp_f32 v77, v77\n\t"
        "v_exp_f32 v78, v78\n\t"
        "v_exp_f32 v79, v79\n\t"
        "v_exp_f32 v80, v80\n\t"
        "v_exp_f32 v81, v81\n\t"
        "v_smfmac_f32_32x32x32_bf16 v[176:191], v[116:119], v[72:79], v7\n\t"
        "ds_read_b128 v[156:159], v12 offset:11264\n\t"
        "v_exp_f32 v82, v82\n\t"
        "v_exp_f32 v83, v83\n\t"
        "v_exp_f32 v84, v84\n\t"
        "v_exp_f32 v85, v85\n\t"
        "v_exp_f32 v86, v86\n\t"
        "v_exp_f32 v87, v87\n\t"
        "v_exp_f32 v88, v88\n\t"
        "v_exp_f32 v89, v89\n\t"
        "s_waitcnt lgkmcnt(0)\n\t"
        "v_readlane_b32 s24, v13, 63\n\t"
        "v_smfmac_f32_32x32x32_bf16 v[176:191], v[120:123], v[80:87], v7\n\t"
        "s_cmp_lg_u32 s24, 0\n\t"
        "s_cbranch_scc0 L_rx2%=\n\t"
        "L_got2%=:\n\t"
        "s_setprio 3\n\t"
        "v_exp_f32 v90, v90\n\t"
        "v_exp_f32 v91, v91\n\t"
        "v_exp_f32 v92, v92\n\t"
        "v_exp_f32 v93, v93\n\t"
        "v_exp_f32 v94, v94\n\t"
        "v_exp_f32 v95, v95\n\t"
        "v_readlane_b32 s25, v15, 63\n\t"
        "v_add_f32 v140, v140, v156\n\t"
        "v_smfmac_f32_32x32x32_bf16 v[176:191], v[124:127], v[88:95], v7\n\t"
        "s_cmp_lt_u32 s20, 4\n\t"
        "v_add_f32 v141, v141, v157\n\t"
        "v_add_f32 v128, v128, v144\n\t"
        "v_add_f32 v129, v129, v145\n\t"
        "v_add_f32 v130, v130, v146\n\t"
        "v_add_f32 v131, v131, v147\n\t"
        "v_add_f32 v132, v132, v148\n\t"
        "v_add_f32 v133, v133, v149\n\t"
        "v_add_f32 v134, v134, v150\n\t"
        "v_add_f32 v135, v135, v151\n\t"
        "v_pk_add_f32 v[136:137], v[136:137], v[152:153]\n\t"
        "v_pk_add_f32 v[138:139], v[138:139], v[154:155]\n\t"
        "v_pk_add_f32 v[142:143], v[142:143], v[158:159]\n\t"
        "s_cbranch_scc1 L_fin%=\n\t"
        "s_cmp_lg_u32 s25, 0\n\t"
        "s_cbranch_scc0 L_rx3%=\n\t"
        "L_got3%=:\n\t"
        "v_pk_add_f32 v[188:189], v[188:189], v[140:141]\n\t"
        "v_pk_add_f32 v[188:189], v[188:189], v[172:173]\n\t"
        "v_mov_b32 v64, v188\n\t"
        "v_mov_b32 v65, v188\n\t"
        "v_pk_add_f32 v[190:191], v[190:191], v[142:143]\n\t"
        "v_pk_add_f32 v[190:191], v[190:191], v[174:175]\n\t"
        "v_permlane32_swap_b32 v64, v65\n\t"
        "v_pk_add_f32 v[176:177], v[176:177], v[128:129]\n\t"
        "v_pk_add_f32 v[178:179], v[178:179], v[130:131]\n\t"
        "v_rcp_f32 v64, v65\n\t"
        "v_pk_add_f32 v[176:177], v[176:177], v[160:161]\n\t"
        "v_pk_add_f32 v[178:179], v[178:179], v[162:163]\n\t"
        "v_pk_mul_f32 v[188:189], v[188:189], v[64:65] op_sel_hi:[1,0]\n\t"
        "v_pk_mul_f32 v[190:191], v[190:191], v[64:65] op_sel_hi:[1,0]\n\t"
        "v_cvt_pk_bf16_f32 v102, v188, v189\n\t"
        "v_cvt_pk_bf16_f32 v103, v190, v191\n\t"
        "v_pk_add_f32 v[180:181], v[180:181], v[132:133]\n\t"
        "v_pk_add_f32 v[182:183], v[182:183], v[134:135]\n\t"
        "v_pk_add_f32 v[180:181], v[180:181], v[164:165]\n\t"
        "v_pk_add_f32 v[182:183], v[182:183], v[166:167]\n\t"
        "v_pk_mul_f32 v[180:181], v[180:181], v[64:65] op_sel_hi:[1,0]\n\t"
        "v_pk_mul_f32 v[182:183], v[182:183], v[64:65] op_sel_hi:[1,0]\n\t"
        "v_cvt_pk_bf16_f32 v98, v180, v181\n\t"
        "v_cvt_pk_bf16_f32 v99, v182, v183\n\t"
        "ds_write2_b64 v14, v[102:103], v[98:99] offset0:6 offset1:2\n\t"
        "v_pk_add_f32 v[184:185], v[184:185], v[136:137]\n\t"
        "v_pk_add_f32 v[186:187], v[186:187], v[138:139]\n\t"
        "v_pk_add_f32 v[184:185], v[184:185], v[168:169]\n\t"
        "v_pk_add_f32 v[186:187], v[186:187], v[170:171]\n\t"
        "v_pk_mul_f32 v[176:177], v[176:177], v[64:65] op_sel_hi:[1,0]\n\t"
        "v_pk_mul_f32 v[178:179], v[178:179], v[64:65] op_sel_hi:[1,0]\n\t"
        "v_cvt_pk_bf16_f32 v96, v176, v177\n\t"
        "v_cvt_pk_bf16_f32 v97, v178, v179\n\t"
        "v_pk_mul_f32 v[184:185], v[184:185], v[64:65] op_sel_hi:[1,0]\n\t"
        "v_pk_mul_f32 v[186:187], v[186:187], v[64:65] op_sel_hi:[1,0]\n\t"
        "v_cvt_pk_bf16_f32 v100, v184, v185\n\t"
        "v_cvt_pk_bf16_f32 v101, v186, v187\n\t"
        "ds_write2_b64 v14, v[96:97], v[100:101] offset0:0 offset1:4\n\t"
        "ds_read_b128 v[104:107], v4 offset:12288\n\t"
        "ds_read_b128 v[108:111], v4 offset:13312\n\t"
        "s_waitcnt lgkmcnt(1)\n\t"
        "buffer_store_dwordx4 v[104:107], v3, s[48:51], s26 offen sc1\n\t"
        "s_mov_b32 exec_hi, 0xffff\n\t"
        "s_waitcnt lgkmcnt(0)\n\t"
        "buffer_store_dwordx4 v[108:111], v3, s[48:51], s26 offen offset:1024 sc1\n\t"
        "s_endpgm\n\t"
        "L_fin%=:\n\t"
        "v_pk_add_f32 v[176:177], v[176:177], v[128:129]\n\t"
        "v_pk_add_f32 v[178:179], v[178:179], v[130:131]\n\t"
        "v_pk_add_f32 v[180:181], v[180:181], v[132:133]\n\t"
        "v_pk_add_f32 v[182:183], v[182:183], v[134:135]\n\t"
        "v_pk_add_f32 v[184:185], v[184:185], v[136:137]\n\t"
        "v_pk_add_f32 v[186:187], v[186:187], v[138:139]\n\t"
        "v_pk_add_f32 v[188:189], v[188:189], v[140:141]\n\t"
        "v_pk_add_f32 v[190:191], v[190:191], v[142:143]\n\t"
        "s_setprio 0\n\t"
        "ds_write_b128 v4, v[176:179] offset:12288\n\t"
        "ds_write_b128 v4, v[180:183] offset:13312\n\t"
        "ds_write_b128 v4, v[184:187] offset:14336\n\t"
        "ds_write_b128 v4, v[188:191] offset:15360\n\t"
        "s_endpgm\n\t"
        "L_spin%=:\n\t"
        "s_sleep 1\n\t"
        "s_mov_b64 exec, 15\n\t"
        "buffer_load_dword v1, v15, s[52:55], s30 offen sc1\n\t"
        "s_mov_b64 exec, -1\n\t"
        "s_waitcnt vmcnt(0)\n\t"
        "v_cmp_ne_u32 vcc, v1, v13\n\t"
        "s_nop 0\n\t"
        "s_or_b32 s24, vcc_lo, s29\n\t"
        "s_and_b32 s24, s24, 15\n\t"
        "s_cmp_eq_u32 s24, 15\n\t"
        "s_cbranch_scc1 L_ready%=\n\t"
        "s_sub_u32 s63, s63, 1\n\t"
        "s_cmp_lg_u32 s63, 0\n\t"
        "s_cbranch_scc1 L_spin%=\n\t"
        "s_branch L_fb%=\n\t"
        "L_rx0%=:\n\t"
        "s_sleep 1\n\t"
        "ds_read_b32 v13, v10 offset:3072\n\t"
        "ds_read_b128 v[128:131], v10 offset:0\n\t"
        "ds_read_b128 v[132:135], v10 offset:1024\n\t"
        "ds_read_b128 v[136:139], v10 offset:2048\n\t"
        "ds_read_b128 v[140:143], v10 offset:3072\n\t"
        "s_waitcnt lgkmcnt(0)\n\t"
        "v_readlane_b32 s24, v13, 63\n\t"
        "s_nop 0\n\t"
        "s_cmp_lg_u32 s24, 0\n\t"
        "s_cbranch_scc0 L_rx0%=\n\t"
        "s_branch L_got0%=\n\t"
        "L_rx1%=:\n\t"
        "s_sleep 1\n\t"
        "ds_read_b32 v13, v11 offset:7168\n\t"
        "ds_read_b128 v[128:131], v11 offset:4096\n\t"
        "ds_read_b128 v[132:135], v11 offset:5120\n\t"
        "ds_read_b128 v[136:139], v11 offset:6144\n\t"
        "ds_read_b128 v[140:143], v11 offset:7168\n\t"
        "s_waitcnt lgkmcnt(0)\n\t"
        "v_readlane_b32 s24, v13, 63\n\t"
        "s_nop 0\n\t"
        "s_cmp_lg_u32 s24, 0\n\t"
        "s_cbranch_scc0 L_rx1%=\n\t"
        "s_branch L_got1%=\n\t"
        "L_rx2%=:\n\t"
        "s_sleep 1\n\t"
        "ds_read_b32 v13, v12 offset:11264\n\t"
        "ds_read_b128 v[144:147], v12 offset:8192\n\t"
        "ds_read_b128 v[148:151], v12 offset:9216\n\t"
        "ds_read_b128 v[152:155], v12 offset:10240\n\t"
        "ds_read_b128 v[156:159], v12 offset:11264\n\t"
        "s_waitcnt lgkmcnt(0)\n\t"
        "v_readlane_b32 s24, v13, 63\n\t"
        "s_nop 0\n\t"
        "s_cmp_lg_u32 s24, 0\n\t"
        "s_cbranch_scc0 L_rx2%=\n\t"
        "s_branch L_got2%=\n\t"
        "L_rx3%=:\n\t"
        "s_setprio 0\n\t"
        "s_sleep 1\n\t"
        "ds_read_b32 v15, v4 offset:31744\n\t"
        "ds_read_b128 v[160:163], v4 offset:28672\n\t"
        "ds_read_b128 v[164:167], v4 offset:29696\n\t"
        "ds_read_b128 v[168:171], v4 offset:30720\n\t"
        "ds_read_b128 v[172:175], v4 offset:31744\n\t"
        "s_waitcnt lgkmcnt(0)\n\t"
        "v_readlane_b32 s25, v15, 63\n\t"
        "s_nop 0\n\t"
        "s_cmp_lg_u32 s25, 0\n\t"
        "s_cbranch_scc0 L_rx3%=\n\t"
        "s_branch L_got3%=\n\t"
        "L_fb%=:\n\t"
        "s_mul_i32 s24, s63, 3584\n\t"
        "s_add_u32 s24, s24, s23\n\t"
        "buffer_load_dwordx4 v[48:51], v2, s[40:43], s24 offen offset:0\n\t"
        "buffer_load_dwordx3 v[52:54], v2, s[40:43], s24 offen offset:16\n\t"
        "buffer_load_dwordx4 v[56:59], v2, s[40:43], s24 offen offset:1792\n\t"
        "buffer_load_dwordx3 v[60:62], v2, s[40:43], s24 offen offset:1808\n\t"
        "v_mov_b64 v[16:17], 0\n\t"
        "v_mov_b64 v[18:19], 0\n\t"
        "v_mov_b64 v[20:21], 0\n\t"
        "v_mov_b64 v[22:23], 0\n\t"
        "v_mov_b64 v[24:25], 0\n\t"
        "v_mov_b64 v[26:27], 0\n\t"
        "v_mov_b64 v[28:29], 0\n\t"
        "v_mov_b64 v[30:31], 0\n\t"
        "v_mov_b64 v[32:33], 0\n\t"
        "v_mov_b64 v[34:35], 0\n\t"
        "v_mov_b64 v[36:37], 0\n\t"
        "v_mov_b64 v[38:39], 0\n\t"
        "v_mov_b64 v[40:41], 0\n\t"
        "v_mov_b64 v[42:43], 0\n\t"
        "v_mov_b64 v[44:45], 0\n\t"
        "v_mov_b64 v[46:47], 0\n\t"
        "s_lshl_b32 s24, s63, 12\n\t"
        "s_add_u32 s24, s24, s31\n\t"
        "s_waitcnt vmcnt(0)\n\t"
        "v_dot2c_f32_bf16 v30, v48, v48\n\t"
        "v_dot2c_f32_bf16 v31, v56, v56\n\t"
        "v_dot2c_f32_bf16 v30, v49, v49\n\t"
        "v_dot2c_f32_bf16 v31, v57, v57\n\t"
        "v_dot2c_f32_bf16 v30, v50, v50\n\t"
        "v_dot2c_f32_bf16 v31, v58, v58\n\t"
        "v_dot2c_f32_bf16 v30, v51, v51\n\t"
        "v_dot2c_f32_bf16 v31, v59, v59\n\t"
        "v_dot2c_f32_bf16 v30, v52, v52\n\t"
        "v_dot2c_f32_bf16 v31, v60, v60\n\t"
        "v_dot2c_f32_bf16 v30, v53, v53\n\t"
        "v_dot2c_f32_bf16 v31, v61, v61\n\t"
        "v_dot2c_f32_bf16 v30, v54, v54\n\t"
        "v_dot2c_f32_bf16 v31, v62, v62\n\t"
        "s_nop 2\n\t"
        "v_permlane32_swap_b32 v30, v31\n\t"
        "v_add_f32 v30, v30, v31\n\t"
        "v_sqrt_f32 v30, v30\n\t"
        "s_nop 0\n\t"
        "v_add_f32 v30, 0x358637bd, v30\n\t"
        "v_rcp_f32 v30, v30\n\t"
        "s_nop 0\n\t"
        "v_cvt_pk_bf16_f32 v8, v30, 0\n\t"
        "v_cvt_pk_bf16_f32 v9, v30, 0\n\t"
        "s_nop 1\n\t"
        "v_permlane32_swap_b32 v8, v9\n\t"
        "v_lshlrev_b32 v46, 16, v8\n\t"
        "v_lshlrev_b32 v47, 16, v9\n\t"
        "v_dot2c_f32_bf16 v16, v48, v8\n\t"
        "v_dot2c_f32_bf16 v17, v48, v46\n\t"
        "v_dot2c_f32_bf16 v18, v49, v8\n\t"
        "v_dot2c_f32_bf16 v19, v49, v46\n\t"
        "v_dot2c_f32_bf16 v20, v50, v8\n\t"
        "v_dot2c_f32_bf16 v21, v50, v46\n\t"
        "v_dot2c_f32_bf16 v22, v51, v8\n\t"
        "v_dot2c_f32_bf16 v23, v51, v46\n\t"
        "v_dot2c_f32_bf16 v24, v52, v8\n\t"
        "v_dot2c_f32_bf16 v25, v52, v46\n\t"
        "v_dot2c_f32_bf16 v26, v53, v8\n\t"
        "v_dot2c_f32_bf16 v27, v53, v46\n\t"
        "v_dot2c_f32_bf16 v28, v54, v8\n\t"
        "v_dot2c_f32_bf16 v29, v54, v46\n\t"
        "v_dot2c_f32_bf16 v32, v56, v9\n\t"
        "v_dot2c_f32_bf16 v33, v56, v47\n\t"
        "v_cvt_pk_bf16_f32 v48, v16, v17\n\t"
        "v_dot2c_f32_bf16 v34, v57, v9\n\t"
        "v_dot2c_f32_bf16 v35, v57, v47\n\t"
        "v_cvt_pk_bf16_f32 v49, v18, v19\n\t"
        "v_dot2c_f32_bf16 v36, v58, v9\n\t"
        "v_dot2c_f32_bf16 v37, v58, v47\n\t"
        "v_cvt_pk_bf16_f32 v50, v20, v21\n\t"
        "v_dot2c_f32_bf16 v38, v59, v9\n\t"
        "v_dot2c_f32_bf16 v39, v59, v47\n\t"
        "v_cvt_pk_bf16_f32 v51, v22, v23\n\t"
        "v_dot2c_f32_bf16 v40, v60, v9\n\t"
        "v_dot2c_f32_bf16 v41, v60, v47\n\t"
        "v_cvt_pk_bf16_f32 v52, v24, v25\n\t"
        "v_dot2c_f32_bf16 v42, v61, v9\n\t"
        "v_dot2c_f32_bf16 v43, v61, v47\n\t"
        "v_cvt_pk_bf16_f32 v53, v26, v27\n\t"
        "v_dot2c_f32_bf16 v44, v62, v9\n\t"
        "v_dot2c_f32_bf16 v45, v62, v47\n\t"
        "v_cvt_pk_bf16_f32 v54, v28, v29\n\t"
        "v_cvt_pk_bf16_f32 v56, v32, v33\n\t"
        "v_cvt_pk_bf16_f32 v57, v34, v35\n\t"
        "v_cvt_pk_bf16_f32 v58, v36, v37\n\t"
        "v_cvt_pk_bf16_f32 v59, v38, v39\n\t"
        "v_cvt_pk_bf16_f32 v60, v40, v41\n\t"
        "v_cvt_pk_bf16_f32 v61, v42, v43\n\t"
        "v_cvt_pk_bf16_f32 v62, v44, v45\n\t"
        "buffer_store_dwordx4 v[48:51], v4, s[52:55], s24 offen offset:0 sc1\n\t"
        "buffer_store_dwordx4 v[52:55], v4, s[52:55], s24 offen offset:1024 sc1\n\t"
        "buffer_store_dwordx4 v[56:59], v4, s[52:55], s24 offen offset:2048 sc1\n\t"
        "buffer_store_dwordx4 v[60:63], v4, s[52:55], s24 offen offset:3072 sc1\n\t"
        "s_waitcnt vmcnt(0)\n\t"
        "s_add_u32 s63, s63, 1\n\t"
        "s_cmp_lt_u32 s63, 4\n\t"
        "s_cbranch_scc1 L_fb%=\n\t"
        "s_branch L_ready%=\n\t"
        "L_generic%=:\n\t"
        "s_waitcnt vmcnt(0) lgkmcnt(0)\n\t"
        "s_setprio 0\n\t"
        : [ka] "+s"(ka)
        : [bid] "s"(bid), [tid] "v"(threadIdx.x), [lds] "s"((unsigned)(uintptr_t)lds)
        : "v1", "v2", "v3", "v4", "v5", "v6", "v7", "v8", "v9", "v10", "v11", "v12", "v13", "v14", "v15", "v16", "v17", "v18", "v19", "v20", "v21", "v22", "v23", "v24", "v25", "v26", "v27", "v28", "v29", "v30", "v31", "v32", "v33", "v34", "v35", "v36", "v37", "v38", "v39", "v40", "v41", "v42", "v43", "v44", "v45", "v46", "v47", "v48", "v49", "v50", "v51", "v52", "v53", "v54", "v55", "v56", "v57", "v58", "v59", "v60", "v61", "v62", "v63", "v64", "v65", "v66", "v67", "v68", "v69", "v70", "v71", "v72", "v73", "v74", "v75", "v76", "v77", "v78", "v79", "v80", "v81", "v82", "v83", "v84", "v85", "v86", "v87", "v88", "v89", "v90", "v91", "v92", "v93", "v94", "v95", "v96", "v97", "v98", "v99", "v100", "v101", "v102", "v103", "v104", "v105", "v106", "v107", "v108", "v109", "v110", "v111", "v112", "v113", "v114", "v115", "v116", "v117", "v118", "v119", "v120", "v121", "v122", "v123", "v124", "v125", "v126", "v127", "v128", "v129", "v130", "v131", "v132", "v133", "v134", "v135", "v136", "v137", "v138", "v139", "v140", "v141", "v142", "v143", "v144", "v145", "v146", "v147", "v148", "v149", "v150", "v151", "v152", "v153", "v154", "v155", "v156", "v157", "v158", "v159", "v160", "v161", "v162", "v163", "v164", "v165", "v166", "v167", "v168", "v169", "v170", "v171", "v172", "v173", "v174", "v175", "v176", "v177", "v178", "v179", "v180", "v181", "v182", "v183", "v184", "v185", "v186", "v187", "v188", "v189", "v190", "v191", "v192", "v193", "v194", "v195", "v196", "v197", "v198", "v199", "v200", "v201", "v202", "v203", "v204", "v205", "v206", "v207", "v208", "v209", "v210", "v211", "v212", "v213", "v214", "v215", "v216", "v217", "v218", "v219", "v220", "v221", "v222", "v223", "v224", "v225", "v226", "v227", "v228", "v229", "v230", "v231", "v232", "v233", "v234", "v235", "v236", "v237", "v238", "v239", "v240", "v241", "v242", "v243", "v244", "v245", "v246", "v247", "v248", "v249", "v250", "v251", "v252", "v253", "v254", "v255", "s16", "s17", "s18", "s19", "s20", "s21", "s22", "s23", "s24", "s25", "s26", "s27", "s28", "s29", "s30", "s31", "s36", "s37", "s38", "s39", "s40", "s41", "s42", "s43", "s44", "s45", "s46", "s47", "s48", "s49", "s50", "s51", "s52", "s53", "s54", "s55", "s56", "s57", "s58", "s59", "s60", "s61", "s62", "s63", "s64", "s65", "vcc", "scc", "memory");
    const int head = bid & 15, qblk = bid >> 4;
    typedef __attribute__((address_space(1))) unsigned* gptr_t;
    const unsigned tw = ((gptr_t)ka[5])[head >> 1];
    generic_path((const unsigned*)(gptr_t)ka[0], (const unsigned*)(gptr_t)ka[2], (const unsigned*)(gptr_t)ka[4],
                 (head & 1) ? bfhi(tw) : bflo(tw), (unsigned*)(gptr_t)ka[6], head, qblk);
}

extern "C" void kernel_launch(void* const* d_in, const int* in_sizes, int n_in, void* d_out, int out_size,
                              void* d_ws, size_t ws_size, hipStream_t stream) {
    const unsigned nb = 16u * 2048u * 28u * 2u, fmt = 0x20000u;
    qkn_kernel<<<256, 512, 0, stream>>>((const unsigned*)d_in[0], nb, fmt, (const unsigned*)d_in[1], nb, fmt,
                                        (const unsigned*)d_in[2], (const unsigned*)d_in[3], (unsigned*)d_out,
                                        (unsigned*)d_ws);
}
```
